```python
import torch

def _softmax(x, axis=-1):
    return torch.softmax(x, dim=axis)
def _gelu(x):
    return torch.nn.functional.gelu(x, approximate="tanh")
def _silu(x):
    return torch.nn.functional.silu(x)
def _sigmoid(x):
    return torch.sigmoid(x)
def _layernorm(x, axis=-1, eps=1e-5):
    m = x.mean(dim=axis, keepdim=True)
    v = x.var(dim=axis, keepdim=True, unbiased=False)
    return (x - m) / torch.sqrt(v + eps)
def _scatter_add(tgt, idx, src, axis=0):
    return tgt.clone().index_add_(axis, idx, src)
def _segment_sum(src, seg, n):
    out = torch.zeros((n,) + src.shape[1:], dtype=src.dtype)
    return out.index_add_(0, seg, src)
def _pad(x, pw):
    flat = tuple(v for pair in reversed(pw) for v in pair)
    return torch.nn.functional.pad(x, flat)

kernel_name = 'recurrent_scan__ssm_conv_s5__glu_gated_reglu__prompt'

def setup_inputs():

    _w = (lambda a: (lambda t: t.to(torch.bfloat16) if t.is_floating_point() else t)(torch.as_tensor(a)))
    def _A_setup():
        B, S, D = 1, 2048, 512
        P = D
        Lambda_re = -0.5 * (lambda _x: torch.exp(torch.as_tensor(_x)))((lambda *s: torch.randn(*s, dtype=torch.bfloat16) * 0.8)(P))
        Lambda_im = (lambda *s: torch.randn(*s, dtype=torch.bfloat16) * 0.8)(P) * 1.0
        B_re = (lambda *s: torch.randn(*s, dtype=torch.bfloat16) * 0.8)(P, D) * (D ** -0.5)
        B_im = (lambda *s: torch.randn(*s, dtype=torch.bfloat16) * 0.8)(P, D) * (D ** -0.5)
        C_re = (lambda *s: torch.randn(*s, dtype=torch.bfloat16) * 0.8)(D, P) * (P ** -0.5)
        C_im = (lambda *s: torch.randn(*s, dtype=torch.bfloat16) * 0.8)(D, P) * (P ** -0.5)
        log_dt = (lambda *s: torch.randn(*s, dtype=torch.bfloat16) * 0.8)(P) * 0.3 - 2.0
        Dskip = (lambda *s: torch.randn(*s, dtype=torch.bfloat16) * 0.8)(D)
        return {
            "x": _w((lambda *s: torch.randn(*s, dtype=torch.bfloat16) * 0.05)(B, S, D)),
            "Lambda_re": _w(Lambda_re),
            "Lambda_im": _w(Lambda_im),
            "B_re": _w(B_re),
            "B_im": _w(B_im),
            "C_re": _w(C_re),
            "C_im": _w(C_im),
            "log_dt": _w(log_dt),
            "Dskip": _w(Dskip),
        }
    _A_in = _A_setup()

    def _B_setup():
        B, S, D, F = 1, 2048, 512, 1376
        return {
            "x": _w((lambda *s: torch.randn(*s, dtype=torch.bfloat16) * 0.05)(B, S, D)),
            "W_gate": _w((lambda *s: torch.randn(*s, dtype=torch.bfloat16) * 0.2)(D, F) * (2.0 / D) ** 0.5),
            "W_up": _w((lambda *s: torch.randn(*s, dtype=torch.bfloat16) * 0.2)(D, F) * (2.0 / D) ** 0.5),
            "W_down": _w((lambda *s: torch.randn(*s, dtype=torch.bfloat16) * 0.2)(F, D) * (1.0 / F) ** 0.5),
        }
    _B_in = _B_setup()

    return {
        "A_x": _A_in["x"],
        "A_Lambda_re": _A_in["Lambda_re"],
        "A_Lambda_im": _A_in["Lambda_im"],
        "A_B_re": _A_in["B_re"],
        "A_B_im": _A_in["B_im"],
        "A_C_re": _A_in["C_re"],
        "A_C_im": _A_in["C_im"],
        "A_log_dt": _A_in["log_dt"],
        "A_Dskip": _A_in["Dskip"],
        "B_x": _B_in["x"],
        "B_W_gate": _B_in["W_gate"],
        "B_W_up": _B_in["W_up"],
        "B_W_down": _B_in["W_down"],
    }

def reference(A_x, A_Lambda_re, A_Lambda_im, A_B_re, A_B_im, A_C_re, A_C_im, A_log_dt, A_Dskip, B_x, B_W_gate, B_W_up, B_W_down):
    def _A_ref(x, Lambda_re, Lambda_im, B_re, B_im, C_re, C_im, log_dt, Dskip):
        Bsz, L, H = x.shape
        Lam = Lambda_re + 1j * Lambda_im
        Bmat = B_re + 1j * B_im
        Cmat = C_re + 1j * C_im
        dt = torch.exp(log_dt)
        Lam_bar = torch.exp(dt * Lam)
        B_bar = ((Lam_bar - 1.0) / Lam)[:, None] * Bmat
        Bu = torch.einsum("bld,pd->blp", x + 0j, B_bar)
        h = torch.zeros_like(Bu[:, 0, :])
        hs = []
        for t in range(L):
            h = Lam_bar[None, :] * h + Bu[:, t, :]
            hs.append(h)
        hseq = torch.stack(hs, 1)
        y = 2.0 * torch.einsum("blp,dp->bld", hseq, Cmat).real + x * Dskip[None, None, :]
        return y

    def _B_ref(x, W_gate, W_up, W_down):
        gate = torch.einsum("bsd,df->bsf", x, W_gate)
        up = torch.einsum("bsd,df->bsf", x, W_up)
        gate = gate * (lambda x, dt: x.to(dt))(gate > 0, gate.dtype)
        h = gate * up
        return torch.einsum("bsf,fd->bsd", h, W_down)

    x = A_x
    xn = _layernorm(x, axis=-1)
    s = _A_ref(xn, A_Lambda_re, A_Lambda_im, A_B_re, A_B_im, A_C_re, A_C_im, A_log_dt, A_Dskip)
    g = _silu(_B_ref(xn, B_W_gate, B_W_up, B_W_down))
    return x + s * g
```

```cpp
#include <hip/hip_runtime.h>
#include <hip/hip_bf16.h>
typedef __hip_bfloat16 bf16;
constexpr int L=2048, D=512, P=512, F=1376;
__device__ inline float b2f(bf16 v){return __bfloat162float(v);}
__device__ inline float rb(float v){return __bfloat162float(__float2bfloat16(v));}

__global__ void ln_k(const bf16* x, bf16* xn){
  int t=blockIdx.x; int lane=threadIdx.x;
  float v[8]; float s=0;
  for(int j=0;j<8;j++){v[j]=b2f(x[t*D+lane*8+j]); s+=v[j];}
  for(int m=32;m>=1;m>>=1) s+=__shfl_xor(s,m);
  float mean=s/D; float q=0;
  for(int j=0;j<8;j++){float d=v[j]-mean; q+=d*d;}
  for(int m=32;m>=1;m>>=1) q+=__shfl_xor(q,m);
  float var=rb(q/D); float mb=rb(mean);
  float sd=rb(sqrtf(rb(var+1e-5f)));
  for(int j=0;j<8;j++) xn[t*D+lane*8+j]=__float2bfloat16(rb(v[j]-mb)/sd);
}
__global__ void bu_k(const bf16* xn,const bf16* Bre,const bf16* Bim,float2* U){
  int t=blockIdx.x; int p=threadIdx.x;
  float a=0,b=0;
  for(int d=0;d<D;d++){float xv=b2f(xn[t*D+d]); a+=xv*b2f(Bre[p*D+d]); b+=xv*b2f(Bim[p*D+d]);}
  U[t*P+p]=make_float2(a,b);
}
__global__ void scan_k(const bf16* lre,const bf16* lim,const bf16* ldt,float2* U){
  int p=blockIdx.x*64+threadIdx.x;
  float dt=rb(expf(b2f(ldt[p]))); float a=b2f(lre[p]), b=b2f(lim[p]);
  float e=expf(dt*a); float lr=e*cosf(dt*b), li=e*sinf(dt*b);
  float nr=lr-1.f, ni=li; float den=a*a+b*b;
  float cr=(nr*a+ni*b)/den, ci=(ni*a-nr*b)/den;
  float hr=0,hi=0;
  for(int t=0;t<L;t++){float2 u=U[t*P+p]; float ur=cr*u.x-ci*u.y, ui=cr*u.y+ci*u.x;
    float r=lr*hr-li*hi+ur, i=lr*hi+li*hr+ui; hr=r;hi=i; U[t*P+p]=make_float2(hr,hi);}
}
__global__ void glu_k(const bf16* xn,const bf16* Wg,const bf16* Wu,bf16* h){
  int t=blockIdx.x;
  for(int f=threadIdx.x;f<F;f+=blockDim.x){
    float g=0,u=0;
    for(int d=0;d<D;d++){float xv=b2f(xn[t*D+d]); g+=xv*b2f(Wg[d*F+f]); u+=xv*b2f(Wu[d*F+f]);}
    g=rb(g);u=rb(u); g=g>0?g:0.f;
    h[t*F+f]=__float2bfloat16(g*u);
  }
}
__global__ void out_k(const bf16* x,const bf16* xn,const float2* H,const bf16* h,const bf16* Cre,const bf16* Cim,const bf16* Dk,const bf16* Wd,float* out){
  int t=blockIdx.x; int d=threadIdx.x;
  float y=0; for(int p=0;p<P;p++){float2 v=H[t*P+p]; y+=v.x*b2f(Cre[d*P+p])-v.y*b2f(Cim[d*P+p]);}
  float f=0; for(int k=0;k<F;k++) f+=b2f(h[t*F+k])*b2f(Wd[k*D+d]);
  f=rb(f); float g=rb(f/(1.f+expf(-f)));
  float s=2.f*y+rb(b2f(xn[t*D+d])*b2f(Dk[d]));
  out[t*D+d]=b2f(x[t*D+d])+s*g;
}
extern "C" void kernel_launch(void* const* d_in, const int* in_sizes, int n_in, void* d_out, int out_size, void* d_ws, size_t ws_size, hipStream_t stream) {
  const bf16* x=(const bf16*)d_in[0];
  char* ws=(char*)d_ws;
  bf16* xn=(bf16*)ws; float2* U=(float2*)(ws+(4<<20)); bf16* h=(bf16*)(ws+(16<<20));
  ln_k<<<L,64,0,stream>>>(x,xn);
  bu_k<<<L,P,0,stream>>>(xn,(const bf16*)d_in[3],(const bf16*)d_in[4],U);
  scan_k<<<P/64,64,0,stream>>>((const bf16*)d_in[1],(const bf16*)d_in[2],(const bf16*)d_in[7],U);
  glu_k<<<L,256,0,stream>>>(xn,(const bf16*)d_in[10],(const bf16*)d_in[11],h);
  out_k<<<L,D,0,stream>>>(x,xn,U,h,(const bf16*)d_in[5],(const bf16*)d_in[6],(const bf16*)d_in[8],(const bf16*)d_in[12],(float*)d_out);
}
```

```cpp
#include <hip/hip_runtime.h>
#include <stdint.h>

#define FUSED 1

namespace {
typedef __attribute__((ext_vector_type(2))) __bf16 bf2;
typedef __attribute__((ext_vector_type(4))) __bf16 bf4;
typedef __attribute__((ext_vector_type(8))) __bf16 bf8;
typedef __attribute__((ext_vector_type(2))) float f2;
typedef __attribute__((ext_vector_type(4))) float f4;
typedef __attribute__((ext_vector_type(2))) unsigned u2;
typedef __attribute__((ext_vector_type(4))) unsigned u4;
typedef __amdgpu_buffer_rsrc_t srd_t;
#define LAS __attribute__((address_space(3)))
#define DEVI __device__ __forceinline__

constexpr int SEQ = 2048, DM = 512, FF = 1376;
constexpr unsigned WS_EPOCH = 0;
constexpr unsigned WS_STATS = 4096;
constexpr unsigned WS_FLF = 32768;
constexpr unsigned WS_FLS = 49152;
constexpr unsigned WS_AGG = 65536;
constexpr unsigned WS_HS = 1u << 20;
constexpr unsigned WS_HF = 5u << 20;
constexpr unsigned WS_END = WS_HF + 32u * 43u * 4096u;
constexpr unsigned OOB = 0x40000000u;
#define STAMP_OFF (64u << 20)
#define NSTAMP 16
constexpr unsigned STG = 49152, L_A = 0, L_SB = 16384, L_WG = 24576, L_WU = 36864;
constexpr unsigned L_TAB = 147456;
constexpr unsigned L_RAW = 151552;
constexpr unsigned L_CS = 152576;
constexpr unsigned L_TR = 155648;
constexpr unsigned RING = 36864;
constexpr int LDS_BYTES = 163840;

struct Params {
  const void *x, *lre, *lim, *bre, *bim, *cre, *cim, *ldt, *dsk, *wg, *wu, *wd;
  float* out;
  char* ws;
};

DEVI srd_t mk_srd(const void* p, unsigned bytes) { return __builtin_amdgcn_make_buffer_rsrc((void*)p, 0, bytes, 0x00020000); }
DEVI void glds16(unsigned m0v, unsigned voff, srd_t srd, unsigned soff) {
  asm volatile("s_mov_b32 m0, %0\n\ts_nop 0\n\tbuffer_load_dwordx4 %1, %2, %3 offen lds" ::"s"(m0v), "v"(voff), "s"(srd), "s"(soff) : "memory");
}
DEVI void glds16_sc1(unsigned m0v, unsigned voff, srd_t srd, unsigned soff) {
  asm volatile("s_mov_b32 m0, %0\n\ts_nop 0\n\tbuffer_load_dwordx4 %1, %2, %3 offen sc1 lds" ::"s"(m0v), "v"(voff), "s"(srd), "s"(soff) : "memory");
}
#define WAITV(n) asm volatile("s_waitcnt vmcnt(" #n ") lgkmcnt(0)" ::: "memory")
#define BAR() do { asm volatile("" ::: "memory"); __builtin_amdgcn_s_barrier(); asm volatile("" ::: "memory"); } while (0)
DEVI float bf_lo(unsigned u) { return __uint_as_float(u << 16); }
DEVI float bf_hi(unsigned u) { return __uint_as_float(u & 0xffff0000u); }
DEVI float rbf(float v) { return (float)(__bf16)v; }
DEVI unsigned pk_bf(float a, float b) { bf2 r = {(__bf16)a, (__bf16)b}; return __builtin_bit_cast(unsigned, r); }
DEVI f4 mfma(bf8 a, bf8 b, f4 c) {
 return __builtin_amdgcn_mfma_f32_16x16x32_bf16(a, b, c, 0, 0, 0); }
DEVI float sum8(bf8 v, float acc) {
  const bf2 one = {(__bf16)1.0f, (__bf16)1.0f};
  acc = __builtin_amdgcn_fdot2_f32_bf16(__builtin_shufflevector(v, v, 0, 1), one, acc, false);
  acc = __builtin_amdgcn_fdot2_f32_bf16(__builtin_shufflevector(v, v, 2, 3), one, acc, false);
  acc = __builtin_amdgcn_fdot2_f32_bf16(__builtin_shufflevector(v, v, 4, 5), one, acc, false);
  acc = __builtin_amdgcn_fdot2_f32_bf16(__builtin_shufflevector(v, v, 6, 7), one, acc, false);
  return acc;
}
DEVI float gsum(float v) {
  unsigned u = __float_as_uint(v);
  auto a = __builtin_amdgcn_permlane16_swap(u, u, false, false);
  v = __uint_as_float(a[0]) + __uint_as_float(a[1]);
  u = __float_as_uint(v);
  auto b = __builtin_amdgcn_permlane32_swap(u, u, false, false);
  return __uint_as_float(b[0]) + __uint_as_float(b[1]);
}
template <int CTRL> DEVI float dpp_add(float v) {
  return v + __uint_as_float(__builtin_amdgcn_update_dpp(0u, __float_as_uint(v), CTRL, 0xf, 0xf, true));
}
DEVI float wave_sum(float v) {
  v = dpp_add<0xB1>(v);
  v = dpp_add<0x4E>(v);
  v = dpp_add<0x141>(v);
  v = dpp_add<0x140>(v);
  return gsum(v);
}
DEVI void gather4(float v, float& g0, float& g1, float& g2, float& g3) {
  unsigned u = __float_as_uint(v);
  auto a = __builtin_amdgcn_permlane16_swap(u, u, false, false);
  auto e = __builtin_amdgcn_permlane32_swap(a[0], a[0], false, false);
  auto o = __builtin_amdgcn_permlane32_swap(a[1], a[1], false, false);
  g0 = __uint_as_float(e[0]); g2 = __uint_as_float(e[1]); g1 = __uint_as_float(o[0]); g3 = __uint_as_float(o[1]);
}
struct cf { float r, i; };
DEVI cf cmul(cf a, cf b) { return {a.r * b.r - a.i * b.i, a.r * b.i + a.i * b.r}; }
DEVI cf cfma(cf a, cf b, cf c) { return {fmaf(a.r, b.r, fmaf(-a.i, b.i, c.r)), fmaf(a.r, b.i, fmaf(a.i, b.r, c.i))}; }

#define STAMP(k) do {} while (0)

constexpr unsigned SPIN_CAP = 1u << 18;

template <int MODE>
__global__ __launch_bounds__(512, 2) void s5_glu_kernel(Params p) {
  __shared__ __attribute__((aligned(16))) char smem_[LDS_BYTES];
  LAS char* smem = (LAS char*)smem_;
  const int tid = threadIdx.x, lane = tid & 63;
  const int w = __builtin_amdgcn_readfirstlane(tid >> 6);
  const int r16 = lane & 15, g = lane >> 4;
  const int bid = blockIdx.x, xcd = bid & 7, slot = bid >> 3;
  const srd_t sWS = mk_srd(p.ws, WS_END);
  unsigned tag = 0;
  if (MODE != 1) STAMP(0); else STAMP(8);

  if constexpr (MODE != 1) {
    const int tb = (xcd >> 1) * 4 + (slot >> 3), cg = (xcd & 1) * 8 + (slot & 7);
    const int wm = w & 1, wn = w >> 1;
    const bool has3 = cg < 11;
    const int fb0 = has3 ? 3 * cg : 33 + 2 * (cg - 11);
    const srd_t sX = mk_srd(p.x, SEQ * DM * 2);
    const srd_t sB = mk_srd(((w >> 1) & 1) ? p.bim : p.bre, DM * DM * 2);
    const srd_t sW = mk_srd(w < 4 ? p.wg : p.wu, DM * FF * 2);
    u4 xrow; unsigned ep, plre, plim, pldt;
    {
      const unsigned rowoff = (unsigned)(tb * 128 + cg * 8 + w) * 1024u;
      const unsigned ch = (unsigned)(cg * 32 + (wn & 1) * 16 + r16) * 2u;
      const srd_t s1 = mk_srd(p.lre, 1024), s2 = mk_srd(p.lim, 1024), s3 = mk_srd(p.ldt, 1024);
      asm volatile("s_nop 4\n\tbuffer_load_dword %0, off, %5, 0 sc1\n\t"
                   "buffer_load_dwordx4 %1, %6, %7, %8 offen\n\t"
                   "buffer_load_ushort %2, %9, %10, 0 offen\n\t"
                   "buffer_load_ushort %3, %9, %11, 0 offen\n\t"
                   "buffer_load_ushort %4, %9, %12, 0 offen"
                   : "=&v"(ep), "=&v"(xrow), "=&v"(plre), "=&v"(plim), "=&v"(pldt)
                   : "s"(sWS), "v"(lane * 16), "s"(sX), "s"(rowoff), "v"(ch), "s"(s1), "s"(s2), "s"(s3)
                   : "memory");
    }
    unsigned vA[2], vB, vW[3];
#pragma unroll
    for (int s = 0; s < 2; ++s) {
      const int rho = 8 * (2 * w + s) + (lane >> 3);
      const int m = (rho >> 4) & 3, r = rho & 15;
      const int tok = (rho & 64) + 16 * (r >> 2) + 4 * m + (r & 3);
      vA[s] = (unsigned)(tb * 128 + tok) * 1024u + (unsigned)(((lane & 7) ^ ((rho >> 1) & 7)) * 16);
    }
    {
      const int row = 8 * w + (lane >> 3);
      const int ch = cg * 32 + (row >> 5) * 16 + (row & 15);
      vB = (unsigned)ch * 1024u + (unsigned)(((lane & 7) ^ ((row >> 1) & 7)) * 16);
    }
#pragma unroll
    for (int s = 0; s < 3; ++s) {
      const int idx = 64 * (3 * (w & 3) + s) + lane;
      const int k = idx / 12, c = idx - 12 * k;
      const int u = (c >> 1) ^ ((k >> 3) & 1);
      vW[s] = (unsigned)(k * (FF * 2) + (fb0 * 32 + 16 * u + 8 * (c & 1)) * 2);
      if (!has3 && u >= 4) vW[s] = OOB;
    }
    const unsigned mA = L_A + 2048u * w, mB = L_SB + 1024u * w, mW = (w < 4 ? L_WG : L_WU) + 3072u * (w & 3);
    auto stage = [&](int s, unsigned sb) {
      glds16(sb + mA, vA[0], sX, 128u * s);
      glds16(sb + mW, vW[0], sW, 64u * FF * 2 * s);
      glds16(sb + mA + 1024, vA[1], sX, 128u * s);
      glds16(sb + mW + 1024, vW[1], sW, 64u * FF * 2 * s);
      glds16(sb + mB, vB, sB, 128u * s);
      glds16(sb + mW + 2048, vW[2], sW, 64u * FF * 2 * s);
    };
    stage(0, 0); stage(1, STG); stage(2, 2 * STG);
    asm volatile("s_waitcnt vmcnt(18)" : "+v"(ep), "+v"(xrow), "+v"(plre), "+v"(plim), "+v"(pldt)::"memory");
    tag = __builtin_amdgcn_readfirstlane(ep) + 1u;
    STAMP(1);
    {
      float v[8];
#pragma unroll
      for (int j = 0; j < 4; ++j) { v[2 * j] = bf_lo(xrow[j]); v[2 * j + 1] = bf_hi(xrow[j]); }
      float s = ((v[0] + v[1]) + (v[2] + v[3])) + ((v[4] + v[5]) + (v[6] + v[7]));
      const float mean = wave_sum(s) * (1.0f / DM);
      float q = 0.f;
#pragma unroll
      for (int j = 0; j < 8; ++j) { const float d = v[j] - mean; q = fmaf(d, d, q); }
      const float var = rbf(wave_sum(q) * (1.0f / DM));
      const float sd = sqrtf(rbf(var + 1e-5f));
      u2 gr = {pk_bf(mean, sd), tag};
      if (lane == 0) __builtin_amdgcn_raw_buffer_store_b64(gr, sWS, WS_STATS + (unsigned)(tb * 128 + cg * 8 + w) * 8u, 0, 16);
    }
    const unsigned aA0 = (unsigned)(r16 * 128 + ((g ^ (r16 >> 1)) * 16));
    const unsigned q4 = r16 >> 2, p4 = lane & 3;
    const unsigned aWb = (unsigned)((8 * g + q4) * 192 + 8 * p4);

    auto stats_step = [&](int s) {
      if (s == 4 && w == 0) glds16_sc1(L_RAW, lane * 16, sWS, WS_STATS + (unsigned)tb * 1024u);
      if (s == 6) {
        u2 gr = *(LAS u2*)(smem + L_RAW + (wm * 64 + lane) * 8);
        if (__builtin_expect(!__all(gr.y == tag), 0)) {
#pragma unroll 1
          for (unsigned spins = 0; spins < SPIN_CAP; ++spins) {
            gr = __builtin_amdgcn_raw_buffer_load_b64(sWS, WS_STATS + (unsigned)(tb * 128 + wm * 64 + lane) * 8u, 0, 16);
            asm volatile("" : "+v"(gr));
            if (__all(gr.y == tag)) break;
            __builtin_amdgcn_s_sleep(1);
          }
        }
        f2 t = {__builtin_amdgcn_rcpf(bf_hi(gr.x)), -bf_lo(gr.x)};
        *(LAS f2*)(smem + L_TAB + w * 512 + lane * 8) = t;
      }
    };
    auto top = [&](int s) {
      if (s == 0) WAITV(12); else if (s < 7) WAITV(6); else WAITV(0);
      BAR();
    };
    auto trread = [&](unsigned addr) -> bf8 {
      bf4 a = __builtin_amdgcn_ds_read_tr16_b64_v4bf16((LAS bf4*)(smem + addr));
      bf4 b = __builtin_amdgcn_ds_read_tr16_b64_v4bf16((LAS bf4*)(smem + addr + 4 * 192));
      return __builtin_shufflevector(a, b, 0, 1, 2, 3, 4, 5, 6, 7);
    };
    const int t64 = tb * 2 + wm;
    auto glu_frag = [&](f4 g0, f4 g1, f4 u0, f4 u1, const f4 (&cs)[2][2], f2 st) -> u4 {
      const float a2 = st.x * st.x;
      float h[8];
#pragma unroll
      for (int j = 0; j < 4; ++j) {
        h[j] = fmaxf(fmaf(st.y, cs[0][0][j], g0[j]), 0.f) * a2 * fmaf(st.y, cs[1][0][j], u0[j]);
        h[4 + j] = fmaxf(fmaf(st.y, cs[0][1][j], g1[j]), 0.f) * a2 * fmaf(st.y, cs[1][1][j], u1[j]);
      }
      return u4{pk_bf(h[0], h[1]), pk_bf(h[2], h[3]), pk_bf(h[4], h[5]), pk_bf(h[6], h[7])};
    };

    if (w >= 4) {
      const int blk = wn - 2;
      f4 acc[2][2][4] = {};
      float cs0 = 0.f, cs1 = 0.f;
      const unsigned aA = aA0 + wm * 8192;
      const unsigned aW0 = L_WG + aWb + (unsigned)(blk * 64 + ((wm ^ (g & 1)) * 32)), aW1 = aW0 ^ 32u;
      unsigned sb = 0;
#pragma unroll 1
      for (int s = 0; s < 8; ++s) {
        top(s);
        if (s == 0) STAMP(2);
#pragma unroll
        for (int kk = 0; kk < 2; ++kk) {
          bf8 a[4], wf[2][2];
#pragma unroll
          for (int m = 0; m < 4; ++m) a[m] = *(LAS bf8*)(smem + sb + (aA ^ (kk * 64)) + m * 2048);
#pragma unroll
          for (int mat = 0; mat < 2; ++mat) {
            wf[mat][0] = trread(sb + aW0 + mat * 12288 + kk * 6144);
            wf[mat][1] = trread(sb + aW1 + mat * 12288 + kk * 6144);
          }
          if (kk == 0) {
            if (s >= 1 && s <= 5) stage(s + 2, sb == 0 ? 2 * STG : sb - STG);
            stats_step(s);
          }
          cs0 = sum8(wf[0][0], cs0); cs1 = sum8(wf[1][0], cs1);
#pragma unroll
          for (int mat = 0; mat < 2; ++mat)
#pragma unroll
            for (int i = 0; i < 2; ++i)
#pragma unroll
              for (int m = 0; m < 4; ++m) acc[mat][i][m] = mfma(wf[mat][i], a[m], acc[mat][i][m]);
        }
        sb = sb == 2 * STG ? 0 : sb + STG;
      }
      STAMP(3);
      cs0 = gsum(cs0); cs1 = gsum(cs1);
      if (g == 0) {
        *(LAS float*)(smem + L_CS + ((blk * 2 + 0) * 2 + wm) * 64 + r16 * 4) = cs0;
        *(LAS float*)(smem + L_CS + ((blk * 2 + 1) * 2 + wm) * 64 + r16 * 4) = cs1;
      }
      WAITV(0); BAR();
      f4 cs[2][2];
#pragma unroll
      for (int mat = 0; mat < 2; ++mat)
#pragma unroll
        for (int i = 0; i < 2; ++i) cs[mat][i] = *(LAS f4*)(smem + L_CS + ((blk * 2 + mat) * 2 + (i ^ wm)) * 64 + g * 16);
      const unsigned so = WS_HF + (unsigned)(t64 * 43 + fb0 + blk) * 4096u;
#pragma unroll
      for (int m = 0; m < 4; ++m) {
        const f2 st = *(LAS f2*)(smem + L_TAB + w * 512 + (16 * (r16 >> 2) + 4 * m + (r16 & 3)) * 8);
        const u4 hv = glu_frag(acc[0][0][m], acc[0][1][m], acc[1][0][m], acc[1][1][m], cs, st);
        __builtin_amdgcn_raw_buffer_store_b128(hv, sWS, lane * 16 + m * 1024, so, MODE == 2 ? 16 : 0);
      }
      STAMP(4);
      if constexpr (MODE == 2) {
        asm volatile("s_waitcnt vmcnt(0)" ::: "memory");
        if (lane == 0) __builtin_amdgcn_raw_buffer_store_b64(u2{tag, tag}, sWS, WS_FLF + (unsigned)(t64 * 512 + (fb0 + blk) * 8), 0, 16);
      }
      STAMP(5);
    } else {
      cf lam, coef2;
      {
        const float dt = rbf(expf(bf_lo(pldt))), a = bf_lo(plre), b = bf_lo(plim);
        const float e = expf(dt * a);
        float sn, cn; sincosf(dt * b, &sn, &cn);
        lam = {e * cn, e * sn};
        const float nr = lam.r - 1.f, ni = lam.i, den = 2.0f / (a * a + b * b);
        coef2 = {(nr * a + ni * b) * den, (ni * a - nr * b) * den};
      }
      f4 as[2][4] = {}, acc[2][2][2] = {};
      float cs0 = 0.f, cs1 = 0.f, csr = 0.f, csi = 0.f;
      const unsigned aA = (aA0 + wm * 8192) ^ (wn * 4096);
      const unsigned aS = L_SB + aA0 + wn * 4096;
      const unsigned aW0 = L_WG + aWb + (unsigned)(128 + ((wm ^ (g & 1)) * 32)), aW1 = aW0 ^ 32u;
      unsigned sb = 0;
#pragma unroll 1
      for (int s = 0; s < 8; ++s) {
        top(s);
        if (s == 0) STAMP(2);
#pragma unroll
        for (int kk = 0; kk < 2; ++kk) {
          bf8 a[4], wf[2][2], sf[2];
          a[0] = *(LAS bf8*)(smem + sb + (aA ^ (kk * 64)));
          a[1] = *(LAS bf8*)(smem + sb + (aA ^ (kk * 64)) + 2048);
          a[2] = *(LAS bf8*)(smem + sb + (aA ^ (kk * 64 + 4096)));
          a[3] = *(LAS bf8*)(smem + sb + (aA ^ (kk * 64 + 4096)) + 2048);
          sf[0] = *(LAS bf8*)(smem + sb + (aS ^ (kk * 64)));
          sf[1] = *(LAS bf8*)(smem + sb + (aS ^ (kk * 64)) + 2048);
#pragma unroll
          for (int mat = 0; mat < 2; ++mat) {
            wf[mat][0] = trread(sb + aW0 + mat * 12288 + kk * 6144);
            wf[mat][1] = trread(sb + aW1 + mat * 12288 + kk * 6144);
          }
          if (kk == 0) {
            if (s >= 1 && s <= 5) stage(s + 2, sb == 0 ? 2 * STG : sb - STG);
            stats_step(s);
          }
          csr = sum8(sf[0], csr); csi = sum8(sf[1], csi);
          cs0 = sum8(wf[0][0], cs0); cs1 = sum8(wf[1][0], cs1);
#pragma unroll
          for (int ri = 0; ri < 2; ++ri)
#pragma unroll
            for (int m = 0; m < 4; ++m) as[ri][m] = mfma(a[m], sf[ri], as[ri][m]);
#pragma unroll
          for (int mat = 0; mat < 2; ++mat)
#pragma unroll
            for (int i = 0; i < 2; ++i)
#pragma unroll
              for (int m = 0; m < 2; ++m) acc[mat][i][m] = mfma(wf[mat][i], a[m], acc[mat][i][m]);
        }
        sb = sb == 2 * STG ? 0 : sb + STG;
      }
      STAMP(3);
      cs0 = gsum(cs0); cs1 = gsum(cs1);
      if (g == 0 && wn == 0) {
        *(LAS float*)(smem + L_CS + ((2 * 2 + 0) * 2 + wm) * 64 + r16 * 4) = cs0;
        *(LAS float*)(smem + L_CS + ((2 * 2 + 1) * 2 + wm) * 64 + r16 * 4) = cs1;
      }
      WAITV(0); BAR();
      csr = gsum(csr); csi = gsum(csi);
      if (wn) {
#pragma unroll
        for (int ri = 0; ri < 2; ++ri) { f4 t0 = as[ri][0], t1 = as[ri][1]; as[ri][0] = as[ri][2]; as[ri][1] = as[ri][3]; as[ri][2] = t0; as[ri][3] = t1; }
      }
      cf agg = {0.f, 0.f};
#pragma unroll
      for (int m = 0; m < 4; ++m) {
        const f4 s01 = *(LAS f4*)(smem + L_TAB + w * 512 + (16 * g + 4 * m) * 8);
        const f4 s23 = *(LAS f4*)(smem + L_TAB + w * 512 + (16 * g + 4 * m + 2) * 8);
        const float ra[4] = {s01[0], s01[2], s23[0], s23[2]}, nm[4] = {s01[1], s01[3], s23[1], s23[3]};
#pragma unroll
        for (int j = 0; j < 4; ++j) {
          as[0][m][j] = ra[j] * fmaf(nm[j], csr, as[0][m][j]);
          as[1][m][j] = ra[j] * fmaf(nm[j], csi, as[1][m][j]);
          agg = cfma(lam, agg, cf{as[0][m][j], as[1][m][j]});
        }
      }
      cf l2 = cmul(lam, lam), l4 = cmul(l2, l2), l8 = cmul(l4, l4);
      const cf l16 = cmul(l8, l8), l32 = cmul(l16, l16), l64 = cmul(l32, l32), l128 = cmul(l64, l64), l256 = cmul(l128, l128);
      cf A0, A1, A2, A3;
      gather4(agg.r, A0.r, A1.r, A2.r, A3.r);
      gather4(agg.i, A0.i, A1.i, A2.i, A3.i);
      const unsigned ch = (unsigned)(cg * 32 + wn * 16 + r16);
      {
        const cf T = cfma(l16, cfma(l16, cfma(l16, A0, A1), A2), A3);
        if (g == 0) __builtin_amdgcn_raw_buffer_store_b128(u4{__float_as_uint(T.r), tag, __float_as_uint(T.i), tag}, sWS, ch * 16u, WS_AGG + (unsigned)t64 * 8192u, 16);
      }
      STAMP(4);
      if (has3) {
        f4 cs[2][2];
#pragma unroll
        for (int mat = 0; mat < 2; ++mat)
#pragma unroll
          for (int i = 0; i < 2; ++i) cs[mat][i] = *(LAS f4*)(smem + L_CS + ((2 * 2 + mat) * 2 + (i ^ wm)) * 64 + g * 16);
        const unsigned so = WS_HF + (unsigned)(t64 * 43 + fb0 + 2) * 4096u + (unsigned)wn * 2048u;
#pragma unroll
        for (int m = 0; m < 2; ++m) {
          const f2 st = *(LAS f2*)(smem + L_TAB + w * 512 + (16 * (r16 >> 2) + 4 * (2 * wn + m) + (r16 & 3)) * 8);
          const u4 hv = glu_frag(acc[0][0][m], acc[0][1][m], acc[1][0][m], acc[1][1][m], cs, st);
          __builtin_amdgcn_raw_buffer_store_b128(hv, sWS, lane * 16 + m * 1024, so, MODE == 2 ? 16 : 0);
        }
        if constexpr (MODE == 2) {
          asm volatile("s_waitcnt vmcnt(0)" ::: "memory");
          if (lane == 0) __builtin_amdgcn_raw_buffer_store_b32(tag, sWS, WS_FLF + (unsigned)(t64 * 512 + (fb0 + 2) * 8 + wn * 4), 0, 16);
        }
      }
      STAMP(5);
      cf carry;
      {
        u4 X[8];
        unsigned spins = 0;
        for (;;) {
          bool ok = true;
#pragma unroll
          for (int i = 0; i < 8; ++i) {
            const int src = t64 - 1 - (4 * i + g);
            X[i] = __builtin_amdgcn_raw_buffer_load_b128(sWS, src >= 0 ? WS_AGG + (unsigned)src * 8192u + ch * 16u : OOB, 0, 16);
            ok &= (src < 0) | ((X[i].y == tag) & (X[i].w == tag));
          }
          if (__all(ok) || ++spins > SPIN_CAP) break;
          __builtin_amdgcn_s_sleep(1);
        }
        cf c = {__uint_as_float(X[7].x), __uint_as_float(X[7].z)};
#pragma unroll
        for (int i = 6; i >= 0; --i) c = cfma(l256, c, cf{__uint_as_float(X[i].x), __uint_as_float(X[i].z)});
        const cf l192 = cmul(l128, l64);
        const cf pw = g == 0 ? cf{1.f, 0.f} : g == 1 ? l64 : g == 2 ? l128 : l192;
        c = cmul(c, pw);
        carry = {gsum(c.r), gsum(c.i)};
      }
      if (t64 == 31 && cg == 0 && wn == 0 && lane == 0) __builtin_amdgcn_raw_buffer_store_b32(tag, sWS, WS_EPOCH, 0, 16);
      STAMP(6);
      {
        const cf E1 = cfma(l16, carry, A0), E2 = cfma(l16, E1, A1), E3 = cfma(l16, E2, A2);
        cf h = g == 0 ? carry : g == 1 ? E1 : g == 2 ? E2 : E3;
        LAS char* tr = smem + L_TR + w * 2048;
        const unsigned so = WS_HS + (unsigned)(t64 * 32 + cg * 2) * 4096u + (unsigned)wn * 512u;
        const unsigned vo = (unsigned)((g >> 1) * 4096 + (lane & 31) * 16);
#pragma unroll
        for (int mp = 0; mp < 2; ++mp) {
#pragma unroll
          for (int mm = 0; mm < 2; ++mm) {
            const int m = 2 * mp + mm;
            float orr[4], oi[4];
#pragma unroll
            for (int j = 0; j < 4; ++j) {
              h = cfma(lam, h, cf{as[0][m][j], as[1][m][j]});
              orr[j] = coef2.r * h.r - coef2.i * h.i;
              oi[j] = -(coef2.r * h.i + coef2.i * h.r);
            }
            *(LAS u2*)(tr + mm * 1024 + r16 * 32 + g * 8) = u2{pk_bf(orr[0], orr[1]), pk_bf(orr[2], orr[3])};
            *(LAS u2*)(tr + mm * 1024 + 512 + r16 * 32 + g * 8) = u2{pk_bf(oi[0], oi[1]), pk_bf(oi[2], oi[3])};
          }
          asm volatile("" ::: "memory");
#pragma unroll
          for (int mm = 0; mm < 2; ++mm) {
            LAS char* q = tr + mm * 1024 + (8 * g + q4) * 32 + p4 * 8;
            bf4 a = __builtin_amdgcn_ds_read_tr16_b64_v4bf16((LAS bf4*)q);
            bf4 b = __builtin_amdgcn_ds_read_tr16_b64_v4bf16((LAS bf4*)(q + 128));
            bf8 o = __builtin_shufflevector(a, b, 0, 1, 2, 3, 4, 5, 6, 7);
            __builtin_amdgcn_raw_buffer_store_b128(__builtin_bit_cast(u4, o), sWS, vo + (2 * mp + mm) * 1024, so, MODE == 2 ? 16 : 0);
          }
          asm volatile("" ::: "memory");
        }
        if constexpr (MODE == 2) {
          asm volatile("s_waitcnt vmcnt(0)" ::: "memory");
          if (lane == 0) __builtin_amdgcn_raw_buffer_store_b32(tag, sWS, WS_FLS + (unsigned)(t64 * 256 + cg * 8 + wn * 4), 0, 16);
        }
      }
      STAMP(7);
    }
  }

  if constexpr (MODE != 0) {
    constexpr int AUX = MODE == 2 ? 16 : 0;
    const int t64 = xcd * 4 + (slot >> 3), n64 = slot & 7;
    const int q = w & 3;
    f4 acc[4][4] = {};
    if (w >= 4) {
      const srd_t sD = mk_srd(p.wd, FF * DM * 2);
      const unsigned vD = (unsigned)((lane >> 3) * 1024 + n64 * 128 + ((((lane & 7) >> 1) ^ g) * 32) + (lane & 1) * 16);
      const unsigned ring = q * RING;
      const unsigned ks0 = 11 * q;
      auto fill = [&](int j, int slotj) {
#pragma unroll
        for (int i = 0; i < 4; ++i) glds16(ring + slotj * 4096 + i * 1024, vD, sD, ((ks0 + j) * 32 + 8 * (i ^ (2 * (t64 & 1)))) * 1024u);
      };
#pragma unroll
      for (int j = 0; j < 9; ++j) fill(j, j);
      const unsigned q4 = r16 >> 2, p4 = lane & 3;
      const unsigned aD = ring + (4 * g + q4) * 128 + (((2 * g + (q4 >> 1)) & 3) * 32) + p4 * 8;
      auto bfrag = [&](int slotj, int nt) -> bf8 {
        LAS char* ptr = smem + (aD ^ (nt * 32)) + slotj * 4096;
        bf4 a = __builtin_amdgcn_ds_read_tr16_b64_v4bf16((LAS bf4*)ptr);
        bf4 b = __builtin_amdgcn_ds_read_tr16_b64_v4bf16((LAS bf4*)(ptr + 2048));
        return __builtin_shufflevector(a, b, 0, 1, 2, 3, 4, 5, 6, 7);
      };
      bf8 b01[2][4];
      asm volatile("s_waitcnt vmcnt(28)" ::: "memory");
#pragma unroll
      for (int j = 0; j < 2; ++j)
#pragma unroll
        for (int nt = 0; nt < 4; ++nt) b01[j][nt] = bfrag(j, nt);
      asm volatile("s_waitcnt lgkmcnt(0)" : "+v"(b01[0][0]), "+v"(b01[0][1]), "+v"(b01[0][2]), "+v"(b01[0][3]), "+v"(b01[1][0]), "+v"(b01[1][1]), "+v"(b01[1][2]), "+v"(b01[1][3])::"memory");
      fill(9, 0); fill(10, 1);
      STAMP(9);
      if constexpr (MODE == 2) {
        const int nfl = q < 3 ? 22 : 20;
        unsigned spins = 0;
        for (;;) {
          const unsigned f = __builtin_amdgcn_raw_buffer_load_b32(sWS, lane < nfl ? WS_FLF + (unsigned)(t64 * 512 + ks0 * 8 + lane * 4) : OOB, 0, 16);
          if (__all((lane >= nfl) | (f == tag)) || ++spins > SPIN_CAP) break;
          __builtin_amdgcn_s_sleep(1);
        }
        asm volatile("" ::: "memory");
      }
      STAMP(10);
      const unsigned so = WS_HF + (unsigned)(t64 * 43 + ks0) * 4096u;
      u4 af[11][4];
#pragma unroll
      for (int j = 0; j < 11; ++j)
#pragma unroll
        for (int m = 0; m < 4; ++m)
          af[j][m] = __builtin_amdgcn_raw_buffer_load_b128(sWS, lane * 16 + m * 1024, (j == 10 && q == 3) ? OOB : so + j * 4096, AUX);
#pragma unroll
      for (int j = 0; j < 11; ++j) {
        bf8 b[4];
#pragma unroll
        for (int nt = 0; nt < 4; ++nt) b[nt] = j < 2 ? b01[j][nt] : bfrag(j < 9 ? j : j - 9, nt);
        if (j == 1) {
          asm volatile("" : "+v"(af[1][3])::"memory");
        }
#pragma unroll
        for (int nt = 0; nt < 4; ++nt)
#pragma unroll
          for (int m = 0; m < 4; ++m) acc[nt][m] = mfma(b[nt], __builtin_bit_cast(bf8, af[j][m]), acc[nt][m]);
      }
    } else {
      const srd_t sC0 = mk_srd(p.cre, DM * DM * 2), sC1 = mk_srd(p.cim, DM * DM * 2);
      const unsigned vC = (unsigned)((n64 * 64 + r16) * 1024 + q * 256 + g * 16);
      u4 cfr[8][4];
#pragma unroll
      for (int c = 0; c < 4; ++c)
#pragma unroll
        for (int nt = 0; nt < 4; ++nt) {
          cfr[2 * c][nt] = __builtin_amdgcn_raw_buffer_load_b128(sC0, vC + c * 64, nt * 16384, 0);
          cfr[2 * c + 1][nt] = __builtin_amdgcn_raw_buffer_load_b128(sC1, vC + c * 64, nt * 16384, 0);
        }
      STAMP(9);
      if constexpr (MODE == 2) {
        unsigned spins = 0;
        for (;;) {
          const unsigned f = __builtin_amdgcn_raw_buffer_load_b32(sWS, lane < 8 ? WS_FLS + (unsigned)(t64 * 256 + q * 32 + lane * 4) : OOB, 0, 16);
          if (__all((lane >= 8) | (f == tag)) || ++spins > SPIN_CAP) break;
          __builtin_amdgcn_s_sleep(1);
        }
        asm volatile("" ::: "memory");
      }
      STAMP(10);
      const unsigned so = WS_HS + (unsigned)(t64 * 32 + q * 8) * 4096u;
#pragma unroll
      for (int j = 0; j < 8; ++j) {
        u4 af[4];
#pragma unroll
        for (int m = 0; m < 4; ++m) af[m] = __builtin_amdgcn_raw_buffer_load_b128(sWS, lane * 16 + m * 1024, so + j * 4096, AUX);
#pragma unroll
        for (int nt = 0; nt < 4; ++nt)
#pragma unroll
          for (int m = 0; m < 4; ++m) acc[nt][m] = mfma(__builtin_bit_cast(bf8, cfr[j][nt]), __builtin_bit_cast(bf8, af[m]), acc[nt][m]);
      }
    }
    STAMP(11);
    const srd_t sX2 = mk_srd(p.x, SEQ * DM * 2), sK = mk_srd(p.dsk, DM * 2);
    const int ec = tid & 15;
    u2 xv[2]; unsigned stv[2]; int tok[2];
#pragma unroll
    for (int i = 0; i < 2; ++i) {
      const int rho = (tid >> 4) + 32 * i, m = rho >> 4, r = rho & 15;
      tok[i] = t64 * 64 + 16 * (r >> 2) + 4 * m + (r & 3);
      xv[i] = __builtin_amdgcn_raw_buffer_load_b64(sX2, (unsigned)(tok[i] * 1024 + n64 * 128 + ec * 8), 0, 0);
      stv[i] = __builtin_amdgcn_raw_buffer_load_b32(sWS, WS_STATS + (unsigned)tok[i] * 8u, 0, AUX);
    }
    const u2 dk = __builtin_amdgcn_raw_buffer_load_b64(sK, (unsigned)(n64 * 128 + ec * 8), 0, 0);
    asm volatile("s_waitcnt lgkmcnt(0)" ::: "memory");
    BAR();
#pragma unroll
    for (int nt = 0; nt < 4; ++nt)
#pragma unroll
      for (int m = 0; m < 4; ++m) *(LAS f4*)(smem + w * 16384 + (16 * m + r16) * 256 + (((4 * nt + g) ^ r16) * 16)) = acc[nt][m];
    asm volatile("s_waitcnt lgkmcnt(0)" ::: "memory");
    BAR();
    STAMP(12);
#pragma unroll
    for (int i = 0; i < 2; ++i) {
      const int rho = (tid >> 4) + 32 * i;
      LAS char* src = smem + rho * 256 + ((ec ^ (rho & 15)) * 16);
      f4 ys = *(LAS f4*)(src), yf = *(LAS f4*)(src + 65536);
#pragma unroll
      for (int k = 1; k < 4; ++k) { ys += *(LAS f4*)(src + k * 16384); yf += *(LAS f4*)(src + 65536 + k * 16384); }
      const float mean = bf_lo(stv[i]), rs = __builtin_amdgcn_rcpf(bf_hi(stv[i]));
      const float xs[4] = {bf_lo(xv[i].x), bf_hi(xv[i].x), bf_lo(xv[i].y), bf_hi(xv[i].y)};
      const float ds[4] = {bf_lo(dk.x), bf_hi(dk.x), bf_lo(dk.y), bf_hi(dk.y)};
      f4 o;
#pragma unroll
      for (int j = 0; j < 4; ++j) {
        const float xn = rbf(rbf(xs[j] - mean) * rs);
        const float s = ys[j] + rbf(xn * ds[j]);
        const float f = rbf(yf[j]);
        const float gt = rbf(f * __builtin_amdgcn_rcpf(1.0f + __expf(-f)));
        o[j] = fmaf(s, gt, xs[j]);
      }
      *(f4*)(p.out + (size_t)tok[i] * DM + n64 * 64 + ec * 4) = o;
    }
    STAMP(13);
  }
}
}

extern "C" void kernel_launch(void* const* d_in, const int* in_sizes, int n_in, void* d_out, int out_size, void* d_ws,
                              size_t ws_size, hipStream_t stream) {
  Params p;
  p.x = d_in[0]; p.lre = d_in[1]; p.lim = d_in[2]; p.bre = d_in[3]; p.bim = d_in[4]; p.cre = d_in[5]; p.cim = d_in[6];
  p.ldt = d_in[7]; p.dsk = d_in[8]; p.wg = d_in[10]; p.wu = d_in[11]; p.wd = d_in[12];
  p.out = (float*)d_out; p.ws = (char*)d_ws;
  s5_glu_kernel<2><<<256, 512, 0, stream>>>(p);
}
```

```cpp
#include <hip/hip_runtime.h>
#include <stdint.h>

#define ROT1 0
#define ROT2 0
#define SLP_SWEEP 4
#define SLP_FLAG 4
#define SLP_FIRST 8
#define PF01 1
#define LPRIO 3
#define NC_EARLY 2
#define LPRIO0 3
#define BULK1 1
#define LATE 2
#define PFC 1
#define PFW 0

namespace {
typedef __attribute__((ext_vector_type(2))) __bf16 bf2;
typedef __attribute__((ext_vector_type(4))) __bf16 bf4;
typedef __attribute__((ext_vector_type(8))) __bf16 bf8;
typedef __attribute__((ext_vector_type(2))) float f2;
typedef __attribute__((ext_vector_type(4))) float f4;
typedef __attribute__((ext_vector_type(2))) unsigned u2;
typedef __attribute__((ext_vector_type(4))) unsigned u4;
typedef __amdgpu_buffer_rsrc_t srd_t;
#define LAS __attribute__((address_space(3)))
#define DEVI __device__ __forceinline__
template <int N> struct IC { static constexpr int v = N; };

constexpr int SEQ = 2048, DM = 512, FF = 1376;
constexpr unsigned WS_EPOCH = 0;
constexpr unsigned WS_STATS = 4096;
constexpr unsigned WS_FLC = 24576;
constexpr unsigned WS_FLF = 32768;
constexpr unsigned WS_FLS = 49152;
constexpr unsigned WS_CSB = 57344;
constexpr unsigned WS_AGG = 65536;
constexpr unsigned WS_CSW = 327680;
constexpr unsigned WS_CT = 1u << 20;
constexpr unsigned WS_HS = 2u << 20;
constexpr unsigned WS_HF = 6u << 20;
constexpr unsigned WS_END = WS_HF + 32u * 43u * 4096u;
constexpr unsigned OOB = 0x40000000u;
#define STAMP_OFF (64u << 20)
#define NSTAMP 16
constexpr unsigned L_X = 0;
constexpr unsigned L_W = 81920;
constexpr unsigned L_SB = 131072;
constexpr unsigned L_RS = 155648;
constexpr unsigned L_NM = 156160;
constexpr unsigned L_PR = 156672;
constexpr unsigned L_CS = 157696;
constexpr unsigned L_CSB = 158464;
constexpr unsigned L_CNT = 158720;
constexpr unsigned L_JUNK = 158976;
constexpr unsigned L_CAR = 159232;
constexpr unsigned L_TR = 159744;
constexpr int LDS_BYTES = 163840;

struct Params {
  const void *x, *lre, *lim, *bre, *bim, *cre, *cim, *ldt, *dsk, *wg, *wu, *wd;
  float* out;
  char* ws;
};

DEVI srd_t mk_srd(const void* p, unsigned bytes) { return __builtin_amdgcn_make_buffer_rsrc((void*)p, 0, bytes, 0x00020000); }
DEVI void glds16(unsigned m0v, unsigned voff, srd_t srd, unsigned soff) {
  asm volatile("s_mov_b32 m0, %0\n\ts_nop 0\n\tbuffer_load_dwordx4 %1, %2, %3 offen lds" ::"s"(m0v), "v"(voff), "s"(srd), "s"(soff) : "memory");
}
DEVI void glds16_sc1(unsigned m0v, unsigned voff, srd_t srd, unsigned soff) {
  asm volatile("s_mov_b32 m0, %0\n\ts_nop 0\n\tbuffer_load_dwordx4 %1, %2, %3 offen sc1 lds" ::"s"(m0v), "v"(voff), "s"(srd), "s"(soff) : "memory");
}
DEVI void glds4(unsigned m0v, unsigned voff, srd_t srd, unsigned soff) {
  asm volatile("s_mov_b32 m0, %0\n\ts_nop 0\n\tbuffer_load_dword %1, %2, %3 offen lds" ::"s"(m0v), "v"(voff), "s"(srd), "s"(soff) : "memory");
}
DEVI void glds4_sc1(unsigned m0v, unsigned voff, srd_t srd, unsigned soff) {
  asm volatile("s_mov_b32 m0, %0\n\ts_nop 0\n\tbuffer_load_dword %1, %2, %3 offen sc1 lds" ::"s"(m0v), "v"(voff), "s"(srd), "s"(soff) : "memory");
}
template <int N> DEVI void waitv() { asm volatile("s_waitcnt vmcnt(%0) lgkmcnt(0)" ::"i"(N) : "memory"); }
#define BAR() do { asm volatile("" ::: "memory"); __builtin_amdgcn_s_barrier(); asm volatile("" ::: "memory"); } while (0)
#define SB0() __builtin_amdgcn_sched_barrier(0)
DEVI float bf_lo(unsigned u) { return __uint_as_float(u << 16); }
DEVI float bf_hi(unsigned u) { return __uint_as_float(u & 0xffff0000u); }
DEVI float rbf(float v) { return (float)(__bf16)v; }
DEVI unsigned pk_bf(float a, float b) { bf2 r = {(__bf16)a, (__bf16)b}; return __builtin_bit_cast(unsigned, r); }
DEVI f4 mfma(bf8 a, bf8 b, f4 c) { return __builtin_amdgcn_mfma_f32_16x16x32_bf16(a, b, c, 0, 0, 0); }
DEVI void st128(u4 v, srd_t srd, unsigned voff, unsigned soff) {
  __builtin_amdgcn_raw_buffer_store_b128(v, srd, voff, soff, 16);
  asm volatile("s_nop 1" ::"v"(v) : "memory");
}
DEVI bf8 asb(u4 v) { return __builtin_bit_cast(bf8, v); }
DEVI f2 lo2(f4 v) { return __builtin_shufflevector(v, v, 0, 1); }
DEVI f2 hi2(f4 v) { return __builtin_shufflevector(v, v, 2, 3); }
DEVI f2 fma2(f2 a, f2 b, f2 c) { return __builtin_elementwise_fma(a, b, c); }
DEVI f2 sp(float a) { return f2{a, a}; }
DEVI u2 swap16(unsigned a) { auto r = __builtin_amdgcn_permlane16_swap(a, a, false, false); return u2{r[0], r[1]}; }
DEVI u2 swap32(unsigned a) { auto r = __builtin_amdgcn_permlane32_swap(a, a, false, false); return u2{r[0], r[1]}; }
DEVI float hsum(float v) {
  const unsigned u = __float_as_uint(v);
  auto b = swap32(u);
  return __uint_as_float(b[0]) + __uint_as_float(b[1]);
}
DEVI float gsum(float v) {
  const unsigned u = __float_as_uint(v);
  auto a = swap16(u);
  return hsum(__uint_as_float(a[0]) + __uint_as_float(a[1]));
}
template <int CTRL> DEVI float dpp_add(float v) {
  return v + __uint_as_float(__builtin_amdgcn_update_dpp(0u, __float_as_uint(v), CTRL, 0xf, 0xf, true));
}
DEVI float wave_sum(float v) {
  v = dpp_add<0xB1>(v);
  v = dpp_add<0x4E>(v);
  v = dpp_add<0x141>(v);
  v = dpp_add<0x140>(v);
  return gsum(v);
}
DEVI void gather4(float v, float& g0, float& g1, float& g2, float& g3) {
  unsigned u = __float_as_uint(v);
  auto a = swap16(u);
  auto e = swap32(a[0]);
  auto o = swap32(a[1]);
  g0 = __uint_as_float(e[0]); g2 = __uint_as_float(e[1]); g1 = __uint_as_float(o[0]); g3 = __uint_as_float(o[1]);
}
struct cf { float r, i; };
DEVI float opq(float v) { asm("" : "+v"(v)); return v; }
DEVI cf cmul(cf a, cf b) { return {fmaf(a.r, b.r, opq(-a.i * b.i)), fmaf(a.r, b.i, a.i * b.r)}; }
DEVI cf csq(cf a) { return {fmaf(a.r, a.r, opq(-a.i * a.i)), opq(a.r + a.r) * a.i}; }
DEVI cf cfma(cf a, cf b, cf c) { return {fmaf(a.r, b.r, opq(fmaf(-a.i, b.i, c.r))), fmaf(a.r, b.i, fmaf(a.i, b.r, c.i))}; }
DEVI int tokmap(int m, int r) { return 16 * (r >> 2) + 8 * (r & 1) + 2 * m + ((r >> 1) & 1); }
DEVI void sincos_cw(float x, float& s, float& c) {
  const float k = rintf(x * 0.63661977236758134f);
  float r = fmaf(k, -1.57079625129699707031f, x);
  r = fmaf(k, -7.54978941586159635335e-08f, r);
  r = fmaf(k, -5.39030285815811905290e-15f, r);
  const float z = r * r;
  float ps = fmaf(z, -1.9515295891e-4f, 8.3321608736e-3f);
  ps = fmaf(z, ps, -1.6666654611e-1f);
  const float sn = fmaf(r * z, ps, r);
  float pc = fmaf(z, 2.443315711809948e-5f, -1.388731625493765e-3f);
  pc = fmaf(z, pc, 4.166664568298827e-2f);
  const float cn = fmaf(z * z, pc, fmaf(z, -0.5f, 1.0f));
  const int q = (int)k;
  const float a = (q & 1) ? cn : sn, b = (q & 1) ? sn : cn;
  s = (q & 2) ? -a : a;
  c = ((q + 1) & 2) ? -b : b;
}

#define STAMP(k) do {} while (0)
#define STAMPC(k) do {} while (0)
#define STAMP_OUT() do {} while (0)

constexpr unsigned SPIN_CAP = 1u << 18;

#define WINF 5
#define WINS 3
DEVI unsigned lds_ld(LAS char* a) { return __hip_atomic_load((LAS unsigned*)a, __ATOMIC_RELAXED, __HIP_MEMORY_SCOPE_WORKGROUP); }
DEVI void lds_st(LAS char* a, unsigned v) { __hip_atomic_store((LAS unsigned*)a, v, __ATOMIC_RELAXED, __HIP_MEMORY_SCOPE_WORKGROUP); }
DEVI cf sel4(int g, cf a, cf b, cf c, cf d) { return {g == 0 ? a.r : g == 1 ? b.r : g == 2 ? c.r : d.r, g == 0 ? a.i : g == 1 ? b.i : g == 2 ? c.i : d.i}; }
struct Ssm { cf lam, coef2, l8, l16, l64; };
DEVI Ssm ssm_consts(unsigned plre, unsigned plim, unsigned pldt) {
  Ssm c;
  const float dt = rbf(__expf(bf_lo(pldt))), a = bf_lo(plre), b = bf_lo(plim);
  const float e = __expf(dt * a);
  float sn, cn; sincos_cw(dt * b, sn, cn);
  c.lam = {e * cn, e * sn};
  const float nr = c.lam.r - 1.f, ni = c.lam.i, den = 2.0f * __builtin_amdgcn_rcpf(a * a + b * b);
  c.coef2 = {fmaf(nr, a, opq(ni * b)) * den, fmaf(ni, a, -nr * b) * den};
  c.l8 = csq(csq(csq(c.lam))); c.l16 = csq(c.l8); c.l64 = csq(csq(c.l16));
  return c;
}

#define KARG __attribute__((address_space(4)))
DEVI constexpr unsigned slotoff(int sl) {
  return sl == 0 ? L_X + 4 * 16384u : sl == 1 ? L_X : sl < 5 ? L_W + (sl - 2) * 16384u : sl == 5 ? L_X + 16384u : sl == 6 ? L_SB : L_X + (sl - 5) * 16384u;
}
DEVI constexpr unsigned wslab(int s) { return s < 13 ? L_W + (s & 3) * 12288u : s < 15 ? L_SB + (s - 13) * 12288u : L_X + 3 * 16384u; }
constexpr unsigned L_K = L_X + 4 * 16384u;
#define LANEVARS() \
  int tid = threadIdx.x; asm volatile("" : "+v"(tid)); \
  const int lane = tid & 63, r16 = lane & 15, g = lane >> 4; \
  const unsigned q4 = r16 >> 2, p4 = lane & 3; \
  const unsigned vD = (unsigned)((lane >> 3) * 1024 + n64 * 128 + ((((lane & 7) >> 1) ^ g) * 32) + (lane & 1) * 16); \
  auto fill = [&](int j, int sl) { \
    _Pragma("unroll") for (int i = 0; i < 4; ++i) glds16(slotoff(sl) + q * 4096 + i * 1024, vD, sD, (unsigned)((11 * q + j) * 32 + 8 * i) * 1024u); }; \
  auto pollflags = [&](unsigned base, int n) { \
    _Pragma("unroll 1") for (unsigned spins = 0; spins < SPIN_CAP; ++spins) { \
      const unsigned f = __builtin_amdgcn_raw_buffer_load_b32(sWS, lane < n ? base + lane * 4u : OOB, 0, 16); \
      if (__all((lane >= n) | (f == tag))) break; \
      __builtin_amdgcn_s_sleep(SLP_FLAG); \
    } \
    asm volatile("" ::: "memory"); \
  };
#define PREAMBLE() \
  const KARG Params* kp = (const KARG Params*)__builtin_amdgcn_kernarg_segment_ptr(); asm volatile("" : "+s"(kp)); \
  const KARG Params& p = *kp; \
  int bid = blockIdx.x, w = w_; asm volatile("" : "+s"(bid), "+s"(w)); \
  const int q = w & 3, xcd = bid & 7, slot = bid >> 3; \
  const srd_t sWS = mk_srd(p.ws, WS_END); \
  const int tb = (xcd >> 2) * 8 + (slot >> 2), cg = (xcd & 3) * 4 + (slot & 3); \
  const int t2 = xcd * 4 + (slot >> 3), n64 = slot & 7; \
  const int wm = q & 1, wn = q >> 1, t64 = tb * 2 + wm; \
  const bool has3 = cg < 11; \
  const int fb0 = min(3 * cg, 11 + 2 * cg); \
  const srd_t sX = mk_srd(p.x, SEQ * DM * 2); \
  const srd_t sD = mk_srd(p.wd, FF * DM * 2); \
  const int jmat = bid >= 86, jj = bid - 86 * jmat, jkh = jj >= 43, jln = jj - 43 * jkh; \
  unsigned tag; \
  f4 acc2[4][4] = {};
#define FINAL_LOADS() \
  u2 xv[2]; unsigned stv[2]; \
  _Pragma("unroll") for (int i = 0; i < 2; ++i) { \
    const int tok = t2 * 64 + tokmap((w >> 2) + 2 * i, 4 * q + g); \
    xv[i] = __builtin_amdgcn_raw_buffer_load_b64(sX, (unsigned)(tok * 1024 + n64 * 128 + r16 * 8), 0, 0); \
    stv[i] = __builtin_amdgcn_raw_buffer_load_b32(sWS, WS_STATS + (unsigned)tok * 8u, 0, 16); \
  } \
  const u2 dk = __builtin_amdgcn_raw_buffer_load_b64(mk_srd(p.dsk, DM * 2), (unsigned)(n64 * 128 + r16 * 8), 0, 0);
#define FINAL_PREP() \
  f4 XD[2], XS[2]; \
  _Pragma("unroll") for (int i = 0; i < 2; ++i) { \
    const float mean = bf_lo(stv[i]), rs = __builtin_amdgcn_rcpf(bf_hi(stv[i])); \
    XS[i] = f4{bf_lo(xv[i].x), bf_hi(xv[i].x), bf_lo(xv[i].y), bf_hi(xv[i].y)}; \
    const float ds[4] = {bf_lo(dk.x), bf_hi(dk.x), bf_lo(dk.y), bf_hi(dk.y)}; \
    _Pragma("unroll") for (int j = 0; j < 4; ++j) XD[i][j] = rbf(rbf(rbf(XS[i][j] - mean) * rs) * ds[j]); \
  } \
  asm volatile("" : "+v"(XD[0]), "+v"(XD[1]), "+v"(XS[0]), "+v"(XS[1]));
#define FINAL_OUT() \
  _Pragma("unroll") for (int i = 0; i < 2; ++i) { \
    const int r = 4 * q + g; \
    const unsigned o = (unsigned)(r * 256 + ((r16 ^ r) * 16)); \
    LAS char* ss = smem + (w < 4 ? slotoff(2 + 2 * i) : slotoff(3 + 2 * i)) + o; \
    LAS char* sf = smem + (w < 4 ? slotoff(6 + 2 * i) : slotoff(i ? 0 : 7)) + o; \
    const f4 yf = (*(LAS f4*)(sf) + *(LAS f4*)(sf + 4096)) + (*(LAS f4*)(sf + 8192) + *(LAS f4*)(sf + 12288)); \
    const f4 ys = (*(LAS f4*)(ss) + *(LAS f4*)(ss + 4096)) + (*(LAS f4*)(ss + 8192) + *(LAS f4*)(ss + 12288)); \
    f4 G; \
    _Pragma("unroll") for (int j = 0; j < 4; ++j) { const float f = rbf(yf[j]); G[j] = rbf(f * __builtin_amdgcn_rcpf(1.0f + __expf(-f))); } \
    __builtin_amdgcn_raw_buffer_store_b128(__builtin_bit_cast(u4, (ys + XD[i]) * G + XS[i]), mk_srd(p.out, SEQ * DM * 4), (unsigned)((t2 * 64 + tokmap((w >> 2) + 2 * i, r)) * 2048 + n64 * 256 + r16 * 16), 0, 16); \
  }
DEVI bf8 cat(bf4 a, bf4 b) { return __builtin_shufflevector(a, b, 0, 1, 2, 3, 4, 5, 6, 7); }
DEVI void lds_wait(LAS char* a, unsigned n) { while (lds_ld(a) < n) __builtin_amdgcn_s_sleep(1); asm volatile("" ::: "memory"); }
DEVI void lds_post(LAS char* a) { asm volatile("s_waitcnt lgkmcnt(0)" ::: "memory"); lds_st(a, 1u); }

__global__ __launch_bounds__(512, 2) void s5_glu_kernel(Params p_) {
  __shared__ __attribute__((aligned(16))) char smem_[LDS_BYTES];
  LAS char* smem = (LAS char*)smem_;
  auto rd128 = [&](unsigned addr) -> bf8 { return *(LAS bf8*)(smem + addr); };
  auto rdtr = [&](unsigned addr) -> bf4 { return __builtin_amdgcn_ds_read_tr16_b64_v4bf16((LAS bf4*)(smem + addr)); };
  asm volatile(".Ls5_start:");
  STAMP(0);
  const int w_ = __builtin_amdgcn_readfirstlane(threadIdx.x >> 6);
  if (w_ < 4) {
    PREAMBLE()
    f4 ag[4][4] = {}, av[2][4] = {};
    {
      LANEVARS()
      const int cn64 = bid & 7, cks = bid >> 3;
      u4 wj[4], ct;
      {
        const int brow = (tb >> 3) * 512 + cg * 32 + (tb & 7) * 4 + q, xr = tb * 128 + cg * 8 + 2 * q;
        const srd_t sBB = mk_srd((brow >> 9) ? p.bim : p.bre, DM * DM * 2);
        u4 xa[2];
#pragma unroll
        for (int i = 0; i < 2; ++i) xa[i] = __builtin_amdgcn_raw_buffer_load_b128(sX, lane * 16 + i * 1024, (unsigned)xr * 1024u, 0);
        const u4 br = __builtin_amdgcn_raw_buffer_load_b128(sBB, lane * 16, (unsigned)(brow & 511) * 1024u, 0);
        const unsigned ep = __builtin_amdgcn_raw_buffer_load_b32(sWS, 0, WS_EPOCH, 16);
        const unsigned ch2 = (unsigned)(cg * 32 + wn * 16 + r16) * 2u;
        const unsigned short pa = __builtin_amdgcn_raw_buffer_load_b16(mk_srd(p.lre, 1024), ch2, 0, 0), pb = __builtin_amdgcn_raw_buffer_load_b16(mk_srd(p.lim, 1024), ch2, 0, 0),
                             pc = __builtin_amdgcn_raw_buffer_load_b16(mk_srd(p.ldt, 1024), ch2, 0, 0);
        auto late_loads = [&]() {
          const srd_t sJ = mk_srd(jmat ? p.wu : p.wg, bid < 172 ? DM * FF * 2 : 0);
          const srd_t sC = mk_srd((cks & 1) ? p.cim : p.cre, DM * DM * 2);
          const unsigned vj = (unsigned)((lane >> 3) * 5504 + (lane & 7) * 16), sj = (unsigned)((jkh * 128 + q * 32) * 5504 + jln * 128);
#pragma unroll
          for (int i = 0; i < 4; ++i) wj[i] = __builtin_amdgcn_raw_buffer_load_b128(sJ, vj, sj + i * 8 * 5504, 0);
          ct = __builtin_amdgcn_raw_buffer_load_b128(sC, r16 * 1024 + g * 16, (unsigned)((cn64 * 64 + q * 16) * 1024 + (cks >> 1) * 64), 0);
        };
        if (LATE == 0) late_loads();
        SB0();
        STAMP(1);
        float mean[2], sd[2];
#pragma unroll
        for (int i = 0; i < 2; ++i) {
          float v[8];
#pragma unroll
          for (int j = 0; j < 4; ++j) { v[2 * j] = bf_lo(xa[i][j]); v[2 * j + 1] = bf_hi(xa[i][j]); }
          if (LATE == 1 && i == 0) { asm volatile("" : "+v"(v[0]) :: "memory"); late_loads(); asm volatile("" ::: "memory"); }
          mean[i] = wave_sum(((v[0] + v[1]) + (v[2] + v[3])) + ((v[4] + v[5]) + (v[6] + v[7]))) * (1.0f / DM);
          float qq = 0.f;
#pragma unroll
          for (int j = 0; j < 8; ++j) { const float dd = v[j] - mean[i]; qq = fmaf(dd, dd, qq); }
          sd[i] = __builtin_amdgcn_sqrtf(rbf(rbf(wave_sum(qq) * (1.0f / DM)) + 1e-5f));
        }
        tag = __builtin_amdgcn_readfirstlane(ep) + 1u;
        if (tid < 16) lds_st(smem + L_CNT + tid * 4, tid == 1 ? tag : 0u);
        st128(u4{pk_bf(mean[0], sd[0]), tag, pk_bf(mean[1], sd[1]), tag}, sWS, lane == 0 ? WS_STATS + (unsigned)xr * 8u : OOB, 0);
        float s = 0.f;
#pragma unroll
        for (int j = 0; j < 4; ++j) s += bf_lo(br[j]) + bf_hi(br[j]);
        s = wave_sum(s);
        __builtin_amdgcn_raw_buffer_store_b64(u2{__float_as_uint(s), tag}, sWS, lane == 0 ? WS_CSB + (unsigned)brow * 8u : OOB, 0, 16);
        if (LATE == 2) { asm volatile("" ::: "memory"); late_loads(); }
        SB0();
        if (wm == 0) {
          const Ssm K = ssm_consts(pa, pb, pc);
          const cf l32 = csq(K.l16), l48 = cmul(l32, K.l16), l128 = csq(K.l64), l256 = csq(l128), l384 = cmul(l256, l128);
          const cf l512 = csq(l256), pwg = sel4(g, cf{1.f, 0.f}, l128, l256, l384);
          const cf S0 = cmul(K.coef2, sel4(g, cf{1.f, 0.f}, K.l16, l32, l48)), S1 = cmul(K.l8, S0);
          LAS char* kt = smem + L_K + wn * 8192 + lane * 16;
          *(LAS f4*)(kt) = f4{K.lam.r, K.lam.i, K.coef2.r, K.coef2.i}; *(LAS f4*)(kt + 1024) = f4{K.l8.r, K.l8.i, K.l16.r, K.l16.i};
          *(LAS f4*)(kt + 2048) = f4{K.l64.r, K.l64.i, l512.r, l512.i}; *(LAS f4*)(kt + 3072) = f4{S0.r, S0.i, S1.r, S1.i};
          *(LAS f2*)(kt + 4096) = f2{pwg.r, pwg.i};
        }
      }
      auto late_jobs = [&]() {
        u4 sel;
#pragma unroll
        for (int i = 0; i < 4; ++i) sel[i] = (r16 >> 1) == i ? ((r16 & 1) ? 0x3f800000u : 0x00003f80u) : 0u;
        f4 d = {0.f, 0.f, 0.f, 0.f};
#pragma unroll
        for (int i = 0; i < 4; ++i) d = mfma(asb(wj[i]), asb(sel), d);
        st128(ct, sWS, lane * 16, WS_CT + (unsigned)((cn64 * 32 + cks) * 4 + q) * 1024u);
#pragma unroll
        for (int j = 0; j < 4; ++j) d[j] = hsum(d[j]);
        if ((lane & 0x28) == 0) *(LAS f4*)(smem + L_TR + q * 256 + (g * 8 + r16) * 16) = d;
      };
      unsigned ax[4], aw[4];
      const unsigned aA0 = (unsigned)(r16 * 128 + ((g ^ (r16 >> 1)) * 16));
      ax[0] = (aA0 + wm * 8192) ^ (wn * 4096); ax[1] = ax[0] ^ 64u; ax[2] = ax[0] ^ 4096u; ax[3] = ax[2] ^ 64u;
      aw[2] = (unsigned)((8 * g + q4) * 192 + 8 * p4 + (g & 1) * 32) + 128u; aw[3] = aw[2] ^ 32u;
      aw[0] = aw[2] - 128u + wn * 64u; aw[1] = aw[0] ^ 32u;
#pragma unroll
      for (int i = 0; i < 4; ++i) { aw[i] += L_W; asm volatile("" : "+v"(aw[i]), "+v"(ax[i])); }
      bf8 fa[2][4], fw[4], fv[2][4];
      auto wfrag = [&](int b, int i, int slab) -> bf8 {
        const unsigned a = aw[b + (i & 1)] + (i >> 1) * 6144 + (wslab(slab) - (slab < 13 ? L_W : slab < 15 ? L_SB : wslab(15)));
        return cat(rdtr(a), rdtr(a + 768));
      };
      auto step = [&](auto H_) {
        constexpr int H = decltype(H_)::v, c = H & 1, n = c ^ 1;
        constexpr int kk = (H + 1) & 1;
        if (H == 2) late_jobs();
        if ((H > 0 && H < 10) || H == 11) { waitv<(H == 3 ? 0 : 63)>(); BAR(); }
        if (H == 12 || H == 15) { waitv<63>(); if (lane == 0) __hip_atomic_fetch_add((LAS unsigned*)(smem + L_CNT), 1u, __ATOMIC_RELAXED, __HIP_MEMORY_SCOPE_WORKGROUP); }
        if (H == 3) __builtin_amdgcn_raw_buffer_store_b32(tag, sWS, lane == 0 ? WS_FLC + (unsigned)((cn64 * 32 + cks) * 4 + q) * 4u : OOB, 0, 16);
        SB0();
#pragma unroll
        for (int i = 0; i < 24; ++i) {
          if (i < 16) ag[i & 3][i >> 2] = mfma(fw[i >> 2], fa[c][i & 3], ag[i & 3][i >> 2]);
          else av[i & 1][(i - 16) >> 1] = mfma(fv[c][(i - 16) >> 1], fa[c][i & 1], av[i & 1][(i - 16) >> 1]);
          if (H < 15) {
            if ((i & 3) == 1 && i < 16) fa[n][i >> 2] = rd128(ax[((i >> 2) & 2) + kk] + ((i >> 2) & 1) * 2048);
            else if ((i & 3) == 2 && i < 16) fv[n][i >> 2] = wfrag(2, i >> 2, H + 1);
            else if ((i & 3) == 0 && i >= 4 && i <= 16) fw[(i >> 2) - 1] = wfrag(0, (i >> 2) - 1, H + 1);
            if (kk == 1 && H < 14 && i >= 16 && i < 20) { ax[i - 16] += ((H >> 1) % 5 == 4) ? -4 * 16384 : 16384; asm volatile("" : "+v"(ax[i - 16])); }
            if ((H == 11 || H == 13) && i >= 20) { aw[i - 20] += H == 11 ? L_SB - L_W : wslab(15) - L_SB; asm volatile("" : "+v"(aw[i - 20])); }
          }
          SB0();
        }
      };
      STAMP(2);
      waitv<63>(); BAR();
      STAMP(3);
#pragma unroll
      for (int i = 0; i < 4; ++i) { fw[i] = wfrag(0, i, 0); fa[0][i] = rd128(ax[i & 2] + (i & 1) * 2048); }
#pragma unroll
      for (int i = 0; i < 4; ++i) fv[0][i] = wfrag(2, i, 0);
      step(IC<0>{}); step(IC<1>{}); step(IC<2>{}); step(IC<3>{}); step(IC<4>{}); step(IC<5>{}); step(IC<6>{}); step(IC<7>{});
      STAMP(4);
      step(IC<8>{}); step(IC<9>{}); step(IC<10>{}); step(IC<11>{}); step(IC<12>{}); step(IC<13>{}); step(IC<14>{}); step(IC<15>{});
    }
    STAMP(5);
    LANEVARS()
    {
      auto glu_frag = [&](const f4 (&ac)[4], const f4 (&cs)[4], f2 st) -> u4 {
        const f2 nm = sp(opq(st.y)), a2 = sp(st.x);
        unsigned o[4];
#pragma unroll
        for (int i = 0; i < 2; ++i) {
          f2 g0 = fma2(nm, lo2(cs[i]), lo2(ac[i])), g1 = fma2(nm, hi2(cs[i]), hi2(ac[i]));
          f2 u0 = fma2(nm, lo2(cs[2 + i]), lo2(ac[2 + i])) * a2, u1 = fma2(nm, hi2(cs[2 + i]), hi2(ac[2 + i])) * a2;
          g0 = f2{fmaxf(g0.x, 0.f), fmaxf(g0.y, 0.f)} * u0;
          g1 = f2{fmaxf(g1.x, 0.f), fmaxf(g1.y, 0.f)} * u1;
          o[2 * i] = pk_bf(g0.x, g0.y); o[2 * i + 1] = pk_bf(g1.x, g1.y);
        }
        return u4{o[0], o[1], o[2], o[3]};
      };
      f4 cs[4], cv[4]; f2 st[4];
#pragma unroll
      for (int i = 0; i < 4; ++i) {
        cs[i] = *(LAS f4*)(smem + L_CS + ((i >> 1) * 96 + wn * 32 + (i & 1) * 16 + g * 4) * 4);
        st[i] = *(LAS f2*)(smem + L_PR + (wm * 64 + r16) * 8 + ((i * 128) ^ (wn * 256)));
      }
#pragma unroll
      for (int i = 0; i < 4; ++i) cv[i] = *(LAS f4*)(smem + L_CS + ((i >> 1) * 96 + 64 + (i & 1) * 16 + g * 4) * 4);
      const unsigned so = WS_HF + (unsigned)(t64 * 43 + fb0) * 4096u;
      const unsigned vo = (unsigned)(lane * 16 + wn * 2048);
#pragma unroll
      for (int m = 0; m < 4; ++m) { st128(glu_frag(ag[m], cs, st[m]), sWS, (m < 2 ? vo : vo ^ 2048u) + (m & 1) * 1024, so + wn * 4096); SB0(); }
#pragma unroll
      for (int m = 0; m < 2; ++m) { st128(glu_frag(av[m], cv, st[m]), sWS, vo + m * 1024, has3 ? so + 8192 : OOB); SB0(); }
    }
    STAMP(6);
    const unsigned aD = q * 4096 + (4 * g + q4) * 128 + (((2 * g + (q4 >> 1)) & 3) * 32) + p4 * 8;
    auto bfrag = [&](int sl, int nt) -> bf8 { return cat(rdtr((aD ^ (nt * 32)) + slotoff(sl)), rdtr((aD ^ (nt * 32)) + slotoff(sl) + 2048)); };
    asm volatile("s_waitcnt vmcnt(0)" ::: "memory");
    __builtin_amdgcn_raw_buffer_store_b32(tag, sWS, lane < 2 ? (unsigned)(wn * 8 + lane * 4) : (lane == 2 && has3) ? (unsigned)(16 + wn * 4) : OOB, WS_FLF + (unsigned)(t64 * 512 + fb0 * 8), 16);
    STAMP(7);
    lds_wait(smem + L_CNT, 8u);
    fill(5, 5); fill(6, 6); fill(7, 7); fill(8, 8);
    lds_wait(smem + L_CNT + 32 + q * 4, 1u);
    bf8 b01[2][4];
#pragma unroll
    for (int j = 0; j < 2; ++j)
#pragma unroll
      for (int nt = 0; nt < 4; ++nt) b01[j][nt] = bfrag(j, nt);
    asm volatile("s_waitcnt lgkmcnt(0)" : "+v"(b01[0][0]), "+v"(b01[0][1]), "+v"(b01[0][2]), "+v"(b01[0][3]), "+v"(b01[1][0]), "+v"(b01[1][1]), "+v"(b01[1][2]), "+v"(b01[1][3])::"memory");
    fill(9, 0); fill(10, 1);
    pollflags(WS_FLF + (unsigned)(t2 * 512 + q * 88), q < 3 ? 22 : 20);
    STAMP(8);
    u4 af[11][4];
    auto lda = [&](int j, int m) {
      af[j][m] = __builtin_amdgcn_raw_buffer_load_b128(sWS, lane * 16 + m * 1024, (j < 10 || q < 3) ? WS_HF + (unsigned)(t2 * 43 + 11 * q + j) * 4096u : OOB, 16);
    };
#pragma unroll
    for (int j = 0; j < WINF; ++j)
#pragma unroll
      for (int m = 0; m < 4; ++m) lda(j, m);
    FINAL_LOADS()
    STAMP(9);
    SB0();
    {
      bf8 bc[4], bn[4];
      const unsigned pw = q * 4096 + r16 * 256 + ((g ^ r16) * 16);
#pragma unroll
      for (int nt = 0; nt < 4; ++nt) bc[nt] = b01[0][nt];
#pragma unroll
      for (int j = 0; j < 11; ++j) {
#pragma unroll
        for (int i = 0; i < 16; ++i) {
          acc2[i >> 2][i & 3] = mfma(bc[i >> 2], asb(af[j][i & 3]), acc2[i >> 2][i & 3]);
          if (j + 1 < 11 && i < 4) bn[i] = j + 1 < 2 ? b01[1][i] : bfrag(j + 1 < 9 ? j + 1 : j + 1 - 9, i);
          if (j + WINF < 11 && (i & 3) == 3) lda(j + WINF, i >> 2);
          if (j == 4 && i == 12) lds_post(smem + L_CNT + 16 + q * 4);
          if (j == 10 && i >= 4) { const int k = i - 4; *(LAS f4*)(smem + slotoff((k & 3) < 3 ? 6 + (k & 3) : 0) + (pw ^ ((k >> 2) * 64))) = acc2[k >> 2][k & 3]; }
          SB0();
        }
#pragma unroll
        for (int nt = 0; nt < 4; ++nt) bc[nt] = bn[nt];
      }
      STAMP(10);
#pragma unroll
      for (int k = 12; k < 16; ++k) *(LAS f4*)(smem + slotoff((k & 3) < 3 ? 6 + (k & 3) : 0) + (pw ^ ((k >> 2) * 64))) = acc2[k >> 2][k & 3];
    }
    FINAL_PREP()
    waitv<63>();
    STAMP(11);
    BAR();
    STAMP(12);
    FINAL_OUT()
    STAMP(13);
    STAMP_OUT();
    asm volatile(".Ls5_end_a:");
    __builtin_amdgcn_endpgm();
  } else {
    PREAMBLE()
    LANEVARS()
    {
      unsigned long long pc; unsigned len, back;
      asm volatile("s_getpc_b64 %0\n.Ls5_pf_here:\n\ts_mov_b32 %1, .Ls5_end_a-.Ls5_start\n\ts_max_i32 %1, %1, .Ls5_end_b-.Ls5_start\n\ts_mov_b32 %2, .Ls5_pf_here-.Ls5_start" : "=s"(pc), "=s"(len), "=s"(back)::"scc");
      const srd_t sI = mk_srd((const void*)(pc - back), (PFC && q == 0) ? len : 0u);
      glds4(L_JUNK, (unsigned)((lane * 32 + slot) * 128), sI, 0);
    }
    const srd_t sB = mk_srd((q & 1) ? p.bim : p.bre, DM * DM * 2);
    const srd_t sW = mk_srd(q < 2 ? p.wg : p.wu, DM * FF * 2);
    if (PF01) glds4(L_JUNK, lane < 6 ? (unsigned)((bid * 2 + (q & 1)) * 6 + lane) * 128u : OOB, sW, 0);
    unsigned vA[2], vB[2], vW[3];
#pragma unroll
    for (int i = 0; i < 2; ++i) {
      const int r = 8 * i + (lane >> 3);
      const unsigned sw = (unsigned)(((lane & 7) ^ ((r >> 1) & 7)) * 16);
      vA[i] = (unsigned)(tb * 128 + (q >> 1) * 64 + tokmap(2 * (q & 1), r)) * 1024u + sw;
      vB[i] = (unsigned)(cg * 32 + (q >> 1) * 16 + r) * 1024u + sw;
    }
#pragma unroll
    for (int s = 0; s < 3; ++s) {
      const int idx = 64 * (3 * (q & 1) + s) + lane;
      const int k = idx / 12, c = idx - 12 * k;
      const int u = (c >> 1) ^ ((k >> 3) & 1);
      vW[s] = (unsigned)(k * (FF * 2) + (fb0 * 32 + 16 * u + 8 * (c & 1)) * 2);
      if (!has3 && u >= 4) vW[s] = OOB;
    }
    auto pc1 = [&](int kind, int s, int i) {
      if (kind == 0) glds16(L_X + (s % 5) * 16384 + q * 4096 + i * 1024, vA[i & 1], sX, 128u * s + (i >> 1) * 2048u);
      if (kind == 1) glds16(L_SB + (s % 3) * 8192 + q * 2048 + i * 1024, vB[i], sB, 128u * s);
      if (kind == 2) glds16(wslab(s) + q * 3072 + i * 1024, vW[i], sW, 32u * FF * 2 * s);
    };
    auto ldX = [&](int s) { pc1(0, s, 0); pc1(0, s, 1); pc1(0, s, 2); pc1(0, s, 3); };
    auto ldB = [&](int s) { pc1(1, s, 0); pc1(1, s, 1); };
    auto ldW = [&](int s) { pc1(2, s, 0); pc1(2, s, 1); pc1(2, s, 2); };
    ldX(0); ldB(0); ldW(0); ldW(1);
    STAMP(1);
    ldX(1);
    asm volatile("s_waitcnt vmcnt(4)" ::: "memory");
    STAMP(2);
    BAR();
    __builtin_amdgcn_s_setprio(LPRIO0);
    STAMP(3);
    const unsigned ch = (unsigned)(cg * 32 + wn * 16 + r16);
    const unsigned aA0 = (unsigned)(r16 * 128 + ((g ^ (r16 >> 1)) * 16));
    tag = __builtin_amdgcn_readfirstlane(lds_ld(smem + L_CNT + 4));
    auto poll8 = [&](unsigned addr, bool need) -> u2 {
      u2 gr = {0u, 0u};
#pragma unroll 1
      for (unsigned spins = 0; spins < SPIN_CAP; ++spins) {
        gr = __builtin_amdgcn_raw_buffer_load_b64(sWS, addr, 0, 16);
        asm volatile("" : "+v"(gr));
        if (__all(!need | (gr.y == tag))) break;
        __builtin_amdgcn_s_sleep(SLP_FLAG);
      }
      return gr;
    };
    f4 as[2][4] = {};
    bf8 ra[2][2][4], rb[2][2][2];
    auto s1_read = [&](int s) {
#pragma unroll
      for (int kk = 0; kk < 2; ++kk) {
#pragma unroll
        for (int i = 0; i < 2; ++i) rb[s & 1][kk][i] = rd128(L_SB + (s % 3) * 8192 + wn * 4096 + i * 2048 + (aA0 ^ (kk * 64)));
#pragma unroll
        for (int m = 0; m < 4; ++m) ra[s & 1][kk][m] = rd128(L_X + (s % 5) * 16384 + wm * 8192 + m * 2048 + (aA0 ^ (kk * 64)));
      }
      SB0();
    };
    auto s1_mma = [&](int s, int k0, int s0, int k1, int s1, int k2, int s2, int k3, int s3) {
      const int kind[4] = {k0, k1, k2, k3}, slab[4] = {s0, s1, s2, s3};
      int gi_ = 0, pi_ = 0;
#pragma unroll
      for (int i = 0; i < 16; ++i) {
        if (gi_ < 4 && kind[gi_] >= 0) { pc1(kind[gi_], slab[gi_], pi_); if (++pi_ == (kind[gi_] == 0 ? 4 : kind[gi_] == 1 ? 2 : 3)) { pi_ = 0; ++gi_; } }
        as[(i >> 2) & 1][i & 3] = mfma(ra[s & 1][i >> 3][i & 3], rb[s & 1][i >> 3][(i >> 2) & 1], as[(i >> 2) & 1][i & 3]);
        SB0();
      }
    };
    u4 cfr[8][4];
    auto ldC = [&](int j) {
#pragma unroll
      for (int nt = 0; nt < 4; ++nt) cfr[j][nt] = __builtin_amdgcn_raw_buffer_load_b128(sWS, lane * 16 + nt * 1024, WS_CT + (unsigned)(n64 * 32 + q * 8 + j) * 4096u, 16);
    };
#define NEXT(n) do { waitv<n>(); BAR(); } while (0)
    Ssm K; cf l512, pwg, S0, S1;
    s1_read(0);
    {
      LAS char* kt = smem + L_K + wn * 8192 + lane * 16;
      const f4 k0 = *(LAS f4*)(kt), k1 = *(LAS f4*)(kt + 1024), k2 = *(LAS f4*)(kt + 2048), k3 = *(LAS f4*)(kt + 3072);
      const f2 k4 = *(LAS f2*)(kt + 4096);
      ldB(1); ldW(2); ldX(2); ldB(2); ldW(3); ldX(3);
      K.lam = {k0[0], opq(k0[1])}; K.coef2 = {k0[2], opq(k0[3])}; K.l8 = {k1[0], k1[1]}; K.l16 = {k1[2], k1[3]}; K.l64 = {k2[0], k2[1]};
      l512 = {k2[2], k2[3]}; S0 = {k3[0], k3[1]}; S1 = {k3[2], k3[3]}; pwg = {k4[0], k4[1]};
    }
    NEXT(13);
    s1_read(1); s1_mma(0, 1, 3, 2, 4, 2, 5, 0, 4); NEXT(16);
    s1_read(2);
    {
      float s = 0.f;
#pragma unroll
      for (int i = 0; i < 4; ++i) s += *(LAS float*)(smem + L_TR + i * 256 + ((lane >> 5) * 8 + (lane & 7)) * 16 + ((lane >> 3) & 3) * 4);
      s1_mma(1, 1, 4, 0, 5, 2, 6, -1, 0);
      __builtin_amdgcn_raw_buffer_store_b64(u2{__float_as_uint(s), tag}, sWS, lane * 8, (q == 3 && bid < 172) ? WS_CSW + (unsigned)((jmat * 2 + jkh) * 2752 + jln * 64) * 8u : OOB, 16);
    }
    NEXT(17);
    s1_read(3); s1_mma(2, 1, 5, 0, 6, 2, 7, -1, 0);
    glds4(L_JUNK, lane < 11 ? (unsigned)((bid * 4 + q) * 11 + lane) * 128u : OOB, sD, 0);
    NEXT(18);
    s1_read(4); s1_mma(3, 1, 6, 2, 8, -1, 0, -1, 0); NEXT(13);
    u2 gr[4]; unsigned ga[4], fl;
    const int gi = q * 64 + lane, gmat = gi >= 96, gf = gi - 96 * gmat;
    s1_read(5); s1_mma(4, 1, 7, 0, 7, 2, 9, -1, 0);
#pragma unroll
    for (int s = 0; s < 4; ++s) {
      const unsigned a = WS_CSW + (unsigned)((gmat * 2 + (s >> 1)) * 2752 + (s & 1) * 1376 + fb0 * 32 + gf) * 8u;
      const unsigned b = s < 2 ? WS_STATS + (unsigned)(tb * 128 + s * 64 + tokmap(g, r16)) * 8u : WS_CSB + (unsigned)((lane >> 5) * 512 + cg * 32 + (lane & 31)) * 8u;
      ga[s] = q < 3 ? a : b;
    }
    asm volatile("buffer_load_dwordx2 %0, %5, %9, 0 offen sc1\n\tbuffer_load_dwordx2 %1, %6, %9, 0 offen sc1\n\tbuffer_load_dwordx2 %2, %7, %9, 0 offen sc1\n\tbuffer_load_dwordx2 %3, %8, %9, 0 offen sc1\n\t"
                 "buffer_load_dword %4, %10, %9, 0 offen sc1"
                 : "=&v"(gr[0]), "=&v"(gr[1]), "=&v"(gr[2]), "=&v"(gr[3]), "=&v"(fl)
                 : "v"(ga[0]), "v"(ga[1]), "v"(ga[2]), "v"(ga[3]), "s"(sWS), "v"(WS_FLC + (unsigned)(n64 * 512 + q * 128 + (lane & 31) * 4)) : "memory");
    NEXT(17);
    s1_read(6); s1_mma(5, 2, 10, -1, 0, -1, 0, -1, 0); NEXT(11);
    s1_read(7); s1_mma(6, 2, 11, 2, 15, -1, 0, -1, 0);
    asm volatile("s_waitcnt vmcnt(9)" : "+v"(gr[0]), "+v"(gr[1]), "+v"(gr[2]), "+v"(gr[3]), "+v"(fl)::"memory");
    {
      const bool need = q == 3 || has3 || gf < 64;
      if (__builtin_expect(!__all(!need | ((gr[0].y == tag) & (gr[1].y == tag) & (gr[2].y == tag) & (gr[3].y == tag))), 0)) {
#pragma unroll
        for (int s = 0; s < 4; ++s) gr[s] = poll8(ga[s], need);
      }
      if (q < 3) *(LAS float*)(smem + L_CS + gi * 4) = (__uint_as_float(gr[0].x) + __uint_as_float(gr[1].x)) + (__uint_as_float(gr[2].x) + __uint_as_float(gr[3].x));
      else {
#pragma unroll
        for (int s = 0; s < 2; ++s) {
          const float rs = __builtin_amdgcn_rcpf(bf_hi(gr[s].x)), nm = -bf_lo(gr[s].x);
          *(LAS float*)(smem + L_RS + (s * 64 + lane) * 4) = rs;
          *(LAS float*)(smem + L_NM + (s * 64 + lane) * 4) = rs * nm;
          *(LAS f2*)(smem + L_PR + (s * 64 + lane) * 8) = f2{rs * rs, nm};
        }
        *(LAS unsigned*)(smem + L_CSB + lane * 4) = gr[2].x;
      }
      if (__builtin_expect(!__all(fl == tag), 0)) pollflags(WS_FLC + (unsigned)(n64 * 512 + q * 128), 32);
      asm volatile("" ::: "memory");
    }
    NEXT(9);
    STAMP(4);
    f4 rs[4], br[4], bi[4];
    {
      const float csr = *(LAS float*)(smem + L_CSB + (wn * 16 + r16) * 4), csi = opq(*(LAS float*)(smem + L_CSB + (32 + wn * 16 + r16) * 4));
#pragma unroll
      for (int m = 0; m < 4; ++m) { rs[m] = *(LAS f4*)(smem + L_RS + (wm * 64 + 16 * m + 4 * g) * 4); br[m] = *(LAS f4*)(smem + L_NM + (wm * 64 + 16 * m + 4 * g) * 4); }
      SB0();
      s1_mma(7, 2, 12, 2, 13, 2, 14, -1, 0);
#pragma unroll
      for (int m = 0; m < 4; ++m) { bi[m] = br[m] * csi; br[m] = br[m] * csr; asm volatile("" : "+v"(br[m]), "+v"(bi[m])); }
    }
    NEXT(12);
    __builtin_amdgcn_s_setprio(LPRIO);
    f2 vr[8], vi[8];
    const f2 lr2 = sp(K.lam.r), li2 = sp(K.lam.i);
#pragma unroll
    for (int m = 0; m < 4; ++m) {
      vr[2 * m] = fma2(lo2(rs[m]), lo2(as[0][m]), lo2(br[m])); vr[2 * m + 1] = fma2(hi2(rs[m]), hi2(as[0][m]), hi2(br[m]));
      vi[2 * m] = fma2(lo2(rs[m]), lo2(as[1][m]), lo2(bi[m])); vi[2 * m + 1] = fma2(hi2(rs[m]), hi2(as[1][m]), hi2(bi[m]));
    }
    f2 hr = vr[0], hi = vi[0];
#pragma unroll
    for (int k = 1; k < 8; ++k) {
      const f2 nr = fma2(lr2, hr, fma2(-li2, hi, vr[k])), ni = fma2(lr2, hi, fma2(li2, hr, vi[k]));
      hr = nr; hi = ni;
    }
    const cf runA = {hr.x, hi.x};
    const cf agg = cfma(K.l8, runA, cf{hr.y, hi.y});
    cf A0, A1, A2, A3;
    gather4(agg.r, A0.r, A1.r, A2.r, A3.r);
    gather4(agg.i, A0.i, A1.i, A2.i, A3.i);
    const cf P2 = cfma(K.l16, A0, A1), P3 = cfma(K.l16, P2, A2), T = cfma(K.l16, P3, A3);
    LAS char* car = smem + L_CAR + wn * 256 + r16 * 8;
    if (wm == 0) {
      *(LAS f2*)car = f2{T.r, T.i};
      lds_post(smem + L_CNT + 48 + wn * 4);
      waitv<0>();
    } else {
      lds_wait(smem + L_CNT + 48 + wn * 4, 1u);
      const f2 t0 = *(LAS f2*)car;
      const cf W = cfma(K.l64, cf{t0.x, opq(t0.y)}, T);
      st128(u4{__float_as_uint(W.r), tag, __float_as_uint(W.i), tag}, sWS, g == 0 ? ch * 16u : OOB, WS_AGG + (unsigned)tb * 8192u);
      waitv<1>();
    }
    __builtin_amdgcn_s_setprio(0);
    STAMP(5);
    BAR();
    const f2 cr2 = sp(K.coef2.r), ci2 = sp(K.coef2.i);
#pragma unroll
    for (int k = 0; k < 8; ++k) {
      if (!BULK1) { if (k < 2) fill(k, k); else if (k < 2 + NC_EARLY) ldC(k - 2); }
      const f2 a = fma2(cr2, vr[k], -(ci2 * vi[k])), b = -fma2(cr2, vi[k], ci2 * vr[k]);
      vr[k] = a; vi[k] = b;
      asm volatile("" : "+v"(vr[k]), "+v"(vi[k]));
    }
    cf E0 = cmul(K.coef2, sel4(g, cf{0.f, 0.f}, A0, P2, P3)), E1 = cfma(K.l8, E0, cmul(K.coef2, runA));
    asm volatile("" : "+v"(E0.r), "+v"(E0.i), "+v"(E1.r), "+v"(E1.i));
    if (!BULK1) { lds_wait(smem + L_CNT, 4u); fill(2, 2); fill(3, 3); fill(4, 4); }
    else if (wm == 1) {
      auto fill2 = [&](int j) {
#pragma unroll
        for (int i = 0; i < 8; ++i) glds16(slotoff(j) + (q - (i >> 2)) * 4096 + (i & 3) * 1024, vD, sD, (unsigned)((11 * (q - (i >> 2)) + j) * 32 + 8 * (i & 3)) * 1024u);
      };
      fill2(0); fill2(1);
      lds_wait(smem + L_CNT, 4u);
      fill2(2); fill2(3); fill2(4);
    }
    STAMP(6);
    __builtin_amdgcn_s_setprio(LPRIO);
    cf carry;
    if (wm == 0) {
      u4 Xg[4];
      unsigned ad[4];
#pragma unroll
      for (int i = 0; i < 4; ++i) { const int src = tb - 1 - (4 * i + g); ad[i] = src >= 0 ? WS_AGG + (unsigned)src * 8192u + ch * 16u : OOB; }
      unsigned todo = 0xfu;
#pragma unroll 1
      for (unsigned spins = 0; todo && spins < SPIN_CAP; ++spins) {
#pragma unroll
        for (int i = 0; i < 4; ++i) if (todo >> i & 1) Xg[i] = __builtin_amdgcn_raw_buffer_load_b128(sWS, ad[i], 0, 16);
        unsigned left = 0;
#pragma unroll
        for (int i = 0; i < 4; ++i) left |= __all((ad[i] == OOB) | ((Xg[i].y == tag) & (Xg[i].w == tag))) ? 0u : 1u << i;
        todo = left;
        if (todo) __builtin_amdgcn_s_sleep(SLP_SWEEP);
      }
      cf c = {__uint_as_float(Xg[3].x), __uint_as_float(Xg[3].z)};
#pragma unroll
      for (int i = 2; i >= 0; --i) c = cfma(l512, c, cf{__uint_as_float(Xg[i].x), __uint_as_float(Xg[i].z)});
      c = cmul(c, pwg);
      carry = {gsum(c.r), gsum(c.i)};
      const cf c1 = cfma(K.l64, carry, T);
      *(LAS f2*)(car + 128) = f2{c1.r, c1.i};
      lds_post(smem + L_CNT + 56 + wn * 4);
      if (tb == 15 && cg == 0 && wn == 0 && lane == 0) __builtin_amdgcn_raw_buffer_store_b32(tag, sWS, WS_EPOCH, 0, 16);
    } else {
      lds_wait(smem + L_CNT + 56 + wn * 4, 1u);
      const f2 c1 = *(LAS f2*)(car + 128);
      carry = {c1.x, opq(c1.y)};
    }
    STAMP(7);
    {
      const cf h0 = cfma(S0, carry, E0), h1 = cfma(S1, carry, E1);
      hr = f2{h0.r, h1.r}; hi = f2{-h0.i, -h1.i};
      LAS char* tr = smem + L_TR + q * 1024;
      const unsigned so = WS_HS + (unsigned)(t64 * 32 + cg * 2) * 4096u + (unsigned)wn * 512u;
      const unsigned vo = (unsigned)((g >> 1) * 4096 + (lane & 31) * 16);
      const unsigned qa = L_TR + q * 1024 + (8 * g + q4) * 32 + p4 * 8;
#pragma unroll
      for (int m = 0; m < 4; ++m) {
        unsigned pr[2], pi[2];
#pragma unroll
        for (int j = 0; j < 2; ++j) {
          const int k = 2 * m + j;
          const f2 nr = fma2(lr2, hr, fma2(li2, hi, vr[k])), ni = fma2(lr2, hi, fma2(-li2, hr, vi[k]));
          hr = nr; hi = ni;
          pr[j] = pk_bf(hr.x, hr.y); pi[j] = pk_bf(hi.x, hi.y);
        }
        *(LAS u2*)(tr + r16 * 32 + g * 8) = u2{pr[0], pr[1]};
        *(LAS u2*)(tr + 512 + r16 * 32 + g * 8) = u2{pi[0], pi[1]};
        asm volatile("" ::: "memory");
        const bf8 o = cat(rdtr(qa), rdtr(qa + 128));
        asm volatile("" ::: "memory");
        st128(__builtin_bit_cast(u4, o), sWS, vo + m * 1024, so);
      }
    }
    STAMP(8);
    asm volatile("s_waitcnt vmcnt(0)" ::: "memory");
    __builtin_amdgcn_raw_buffer_store_b32(tag, sWS, lane == 0 ? WS_FLS + (unsigned)(t64 * 256 + cg * 8 + wn * 4) : OOB, 0, 16);
    if (!BULK1) lds_st(smem + L_CNT + 32 + q * 4, 1u);
    else if (wm == 1) { lds_st(smem + L_CNT + 32 + q * 4, 1u); lds_st(smem + L_CNT + 28 + q * 4, 1u); }
    __builtin_amdgcn_s_setprio(0);
    STAMP(9);
#pragma unroll
    for (int j = BULK1 ? 0 : NC_EARLY; j < 8; ++j) ldC(j);
    pollflags(WS_FLS + (unsigned)(t2 * 256 + q * 32), 8);
    STAMP(10);
    u4 af[8][4];
    auto lda = [&](int j, int m) {
      af[j][m] = __builtin_amdgcn_raw_buffer_load_b128(sWS, lane * 16 + m * 1024, WS_HS + (unsigned)(t2 * 32 + q * 8 + j) * 4096u, 16);
    };
#pragma unroll
    for (int j = 0; j < WINS; ++j)
#pragma unroll
      for (int m = 0; m < 4; ++m) lda(j, m);
    SB0();
#pragma unroll
    for (int j = 0; j < 8; ++j) {
#pragma unroll
      for (int i = 0; i < 16; ++i) {
        acc2[i >> 2][i & 3] = mfma(asb(cfr[j][i >> 2]), asb(af[j][i & 3]), acc2[i >> 2][i & 3]);
        if (j >= 1) { const int n = WINS * 4 + (j - 1) * 8 + (i >> 1); if ((i & 1) && n < 32) lda(n >> 2, n & 3); }
        SB0();
      }
    }
    STAMP(11);
    FINAL_LOADS()
    lds_wait(smem + L_CNT + 16 + q * 4, 1u);
#pragma unroll
    for (int nt = 0; nt < 4; ++nt)
#pragma unroll
      for (int m = 0; m < 4; ++m) *(LAS f4*)(smem + slotoff(2 + m) + q * 4096 + r16 * 256 + (((4 * nt + g) ^ r16) * 16)) = acc2[nt][m];
    FINAL_PREP()
    waitv<63>();
    STAMP(12);
    BAR();
    FINAL_OUT()
    STAMP(13);
    STAMP_OUT();
    asm volatile(".Ls5_end_b:");
    __builtin_amdgcn_endpgm();
  }
}
}

extern "C" void kernel_launch(void* const* d_in, const int* in_sizes, int n_in, void* d_out, int out_size, void* d_ws,
                              size_t ws_size, hipStream_t stream) {
  Params p;
  p.x = d_in[0]; p.lre = d_in[1]; p.lim = d_in[2]; p.bre = d_in[3]; p.bim = d_in[4]; p.cre = d_in[5]; p.cim = d_in[6];
  p.ldt = d_in[7]; p.dsk = d_in[8]; p.wg = d_in[10]; p.wu = d_in[11]; p.wd = d_in[12];
  p.out = (float*)d_out; p.ws = (char*)d_ws;
  s5_glu_kernel<<<256, 512, 0, stream>>>(p);
}
```

```cpp
#include <hip/hip_runtime.h>
#include <stdint.h>

#define ROT1 0
#define ROT2 0
#define SLP_SWEEP 4
#define SLP_FLAG 4
#define SLP_FIRST 8
#define PF01 1
#define LPRIO 3
#define NC_EARLY 2
#define LPRIO0 3
#define BULK1 1
#define LATE 2
#define NF_EARLY 5
#define NC_MID 0
#define GATE 0
#define KEEPPRIO 1
#define NOBULK 0
#define PRE_SLEEP 4
#define SWEEP_AT 8
#define CP 3
#define WIN0 3
#define PFC 1
#define PFW 0

namespace {
typedef __attribute__((ext_vector_type(2))) __bf16 bf2;
typedef __attribute__((ext_vector_type(4))) __bf16 bf4;
typedef __attribute__((ext_vector_type(8))) __bf16 bf8;
typedef __attribute__((ext_vector_type(2))) float f2;
typedef __attribute__((ext_vector_type(4))) float f4;
typedef __attribute__((ext_vector_type(2))) unsigned u2;
typedef __attribute__((ext_vector_type(4))) unsigned u4;
typedef __amdgpu_buffer_rsrc_t srd_t;
#define LAS __attribute__((address_space(3)))
#define DEVI __device__ __forceinline__
template <int N> struct IC { static constexpr int v = N; };

constexpr int SEQ = 2048, DM = 512, FF = 1376;
constexpr unsigned WS_EPOCH = 0;
constexpr unsigned WS_STATS = 4096;
constexpr unsigned WS_FLC = 24576;
constexpr unsigned WS_FLF = 524288;
constexpr unsigned WS_FLS = 655360;
constexpr unsigned WS_CSB = 57344;
constexpr unsigned WS_AGG = 65536;
constexpr unsigned WS_CSW = 327680;
constexpr unsigned WS_CT = 1u << 20;
constexpr unsigned WS_HS = 2u << 20;
constexpr unsigned WS_HF = 6u << 20;
constexpr unsigned WS_END = WS_HF + 32u * 43u * 4096u;
constexpr unsigned OOB = 0x40000000u;
#define STAMP_OFF (64u << 20)
#define NSTAMP 16
constexpr unsigned L_X = 0;
constexpr unsigned L_W = 81920;
constexpr unsigned L_SB = 131072;
constexpr unsigned L_RS = 155648;
constexpr unsigned L_NM = 156160;
constexpr unsigned L_PR = 156672;
constexpr unsigned L_CS = 157696;
constexpr unsigned L_CSB = 158464;
constexpr unsigned L_CNT = 158720;
constexpr unsigned L_JUNK = 158976;
constexpr unsigned L_CAR = 159232;
constexpr unsigned L_TR = 159744;
constexpr int LDS_BYTES = 163840;

struct Params {
  const void *x, *lre, *lim, *bre, *bim, *cre, *cim, *ldt, *dsk, *wg, *wu, *wd;
  float* out;
  char* ws;
};

DEVI srd_t mk_srd(const void* p, unsigned bytes) { return __builtin_amdgcn_make_buffer_rsrc((void*)p, 0, bytes, 0x00020000); }
DEVI void glds16(unsigned m0v, unsigned voff, srd_t srd, unsigned soff) {
  asm volatile("s_mov_b32 m0, %0\n\ts_nop 0\n\tbuffer_load_dwordx4 %1, %2, %3 offen lds" ::"s"(m0v), "v"(voff), "s"(srd), "s"(soff) : "memory");
}
DEVI void glds16_sc1(unsigned m0v, unsigned voff, srd_t srd, unsigned soff) {
  asm volatile("s_mov_b32 m0, %0\n\ts_nop 0\n\tbuffer_load_dwordx4 %1, %2, %3 offen sc1 lds" ::"s"(m0v), "v"(voff), "s"(srd), "s"(soff) : "memory");
}
DEVI void glds4(unsigned m0v, unsigned voff, srd_t srd, unsigned soff) {
  asm volatile("s_mov_b32 m0, %0\n\ts_nop 0\n\tbuffer_load_dword %1, %2, %3 offen lds" ::"s"(m0v), "v"(voff), "s"(srd), "s"(soff) : "memory");
}
DEVI void glds4_sc1(unsigned m0v, unsigned voff, srd_t srd, unsigned soff) {
  asm volatile("s_mov_b32 m0, %0\n\ts_nop 0\n\tbuffer_load_dword %1, %2, %3 offen sc1 lds" ::"s"(m0v), "v"(voff), "s"(srd), "s"(soff) : "memory");
}
template <int N> DEVI void waitv() { asm volatile("s_waitcnt vmcnt(%0) lgkmcnt(0)" ::"i"(N) : "memory"); }
#define BAR() do { asm volatile("" ::: "memory"); __builtin_amdgcn_s_barrier(); asm volatile("" ::: "memory"); } while (0)
#define SB0() __builtin_amdgcn_sched_barrier(0)
DEVI float bf_lo(unsigned u) { return __uint_as_float(u << 16); }
DEVI float bf_hi(unsigned u) { return __uint_as_float(u & 0xffff0000u); }
DEVI float rbf(float v) { return (float)(__bf16)v; }
DEVI unsigned pk_bf(float a, float b) { bf2 r = {(__bf16)a, (__bf16)b}; return __builtin_bit_cast(unsigned, r); }
DEVI f4 mfma(bf8 a, bf8 b, f4 c) { return __builtin_amdgcn_mfma_f32_16x16x32_bf16(a, b, c, 0, 0, 0); }
DEVI void st128(u4 v, srd_t srd, unsigned voff, unsigned soff) {
  __builtin_amdgcn_raw_buffer_store_b128(v, srd, voff, soff, 16);
  asm volatile("s_nop 1" ::"v"(v) : "memory");
}
DEVI bf8 asb(u4 v) { return __builtin_bit_cast(bf8, v); }
DEVI f2 lo2(f4 v) { return __builtin_shufflevector(v, v, 0, 1); }
DEVI f2 hi2(f4 v) { return __builtin_shufflevector(v, v, 2, 3); }
DEVI f2 fma2(f2 a, f2 b, f2 c) { return __builtin_elementwise_fma(a, b, c); }
DEVI f2 sp(float a) { return f2{a, a}; }
DEVI u2 swap16(unsigned a) { auto r = __builtin_amdgcn_permlane16_swap(a, a, false, false); return u2{r[0], r[1]}; }
DEVI u2 swap32(unsigned a) { auto r = __builtin_amdgcn_permlane32_swap(a, a, false, false); return u2{r[0], r[1]}; }
DEVI float hsum(float v) {
  const unsigned u = __float_as_uint(v);
  auto b = swap32(u);
  return __uint_as_float(b[0]) + __uint_as_float(b[1]);
}
DEVI float gsum(float v) {
  const unsigned u = __float_as_uint(v);
  auto a = swap16(u);
  return hsum(__uint_as_float(a[0]) + __uint_as_float(a[1]));
}
template <int CTRL> DEVI float dpp_add(float v) {
  return v + __uint_as_float(__builtin_amdgcn_update_dpp(0u, __float_as_uint(v), CTRL, 0xf, 0xf, true));
}
DEVI float wave_sum(float v) {
  v = dpp_add<0xB1>(v);
  v = dpp_add<0x4E>(v);
  v = dpp_add<0x141>(v);
  v = dpp_add<0x140>(v);
  return gsum(v);
}
DEVI void gather4(float v, float& g0, float& g1, float& g2, float& g3) {
  unsigned u = __float_as_uint(v);
  auto a = swap16(u);
  auto e = swap32(a[0]);
  auto o = swap32(a[1]);
  g0 = __uint_as_float(e[0]); g2 = __uint_as_float(e[1]); g1 = __uint_as_float(o[0]); g3 = __uint_as_float(o[1]);
}
struct cf { float r, i; };
DEVI float opq(float v) { asm("" : "+v"(v)); return v; }
DEVI cf cmul(cf a, cf b) { return {fmaf(a.r, b.r, opq(-a.i * b.i)), fmaf(a.r, b.i, a.i * b.r)}; }
DEVI cf csq(cf a) { return {fmaf(a.r, a.r, opq(-a.i * a.i)), opq(a.r + a.r) * a.i}; }
DEVI cf cfma(cf a, cf b, cf c) { return {fmaf(a.r, b.r, opq(fmaf(-a.i, b.i, c.r))), fmaf(a.r, b.i, fmaf(a.i, b.r, c.i))}; }
DEVI int tokmap(int m, int r) { return 16 * (r >> 2) + 8 * (r & 1) + 2 * m + ((r >> 1) & 1); }
DEVI void sincos_cw(float x, float& s, float& c) {
  const float k = rintf(x * 0.63661977236758134f);
  float r = fmaf(k, -1.57079625129699707031f, x);
  r = fmaf(k, -7.54978941586159635335e-08f, r);
  r = fmaf(k, -5.39030285815811905290e-15f, r);
  const float z = r * r;
  float ps = fmaf(z, -1.9515295891e-4f, 8.3321608736e-3f);
  ps = fmaf(z, ps, -1.6666654611e-1f);
  const float sn = fmaf(r * z, ps, r);
  float pc = fmaf(z, 2.443315711809948e-5f, -1.388731625493765e-3f);
  pc = fmaf(z, pc, 4.166664568298827e-2f);
  const float cn = fmaf(z * z, pc, fmaf(z, -0.5f, 1.0f));
  const int q = (int)k;
  const float a = (q & 1) ? cn : sn, b = (q & 1) ? sn : cn;
  s = (q & 2) ? -a : a;
  c = ((q + 1) & 2) ? -b : b;
}

#define STAMP(k) do {} while (0)
#define STAMPC(k) do {} while (0)
#define STAMP_OUT() do {} while (0)

#define FSTAMP(k) do {} while (0)
constexpr unsigned SPIN_CAP = 1u << 18;

#define WINF 5
#define WINS 3
DEVI unsigned lds_ld(LAS char* a) { return __hip_atomic_load((LAS unsigned*)a, __ATOMIC_RELAXED, __HIP_MEMORY_SCOPE_WORKGROUP); }
DEVI void lds_st(LAS char* a, unsigned v) { __hip_atomic_store((LAS unsigned*)a, v, __ATOMIC_RELAXED, __HIP_MEMORY_SCOPE_WORKGROUP); }
DEVI cf sel4(int g, cf a, cf b, cf c, cf d) { return {g == 0 ? a.r : g == 1 ? b.r : g == 2 ? c.r : d.r, g == 0 ? a.i : g == 1 ? b.i : g == 2 ? c.i : d.i}; }
struct Ssm { cf lam, coef2, l8, l16, l64; };
DEVI Ssm ssm_consts(unsigned plre, unsigned plim, unsigned pldt) {
  Ssm c;
  const float dt = rbf(__expf(bf_lo(pldt))), a = bf_lo(plre), b = bf_lo(plim);
  const float e = __expf(dt * a);
  float sn, cn; sincos_cw(dt * b, sn, cn);
  c.lam = {e * cn, e * sn};
  const float nr = c.lam.r - 1.f, ni = c.lam.i, den = 2.0f * __builtin_amdgcn_rcpf(a * a + b * b);
  c.coef2 = {fmaf(nr, a, opq(ni * b)) * den, fmaf(ni, a, -nr * b) * den};
  c.l8 = csq(csq(csq(c.lam))); c.l16 = csq(c.l8); c.l64 = csq(csq(c.l16));
  return c;
}

#define KARG __attribute__((address_space(4)))
DEVI constexpr unsigned slotoff(int sl) {
  return sl == 0 ? L_X + 4 * 16384u : sl == 1 ? L_X : sl < 5 ? L_W + (sl - 2) * 16384u : sl == 5 ? L_X + 16384u : sl == 6 ? L_SB : L_X + (sl - 5) * 16384u;
}
DEVI constexpr unsigned wslab(int s) { return s < 13 ? L_W + (s & 3) * 12288u : s < 15 ? L_SB + (s - 13) * 12288u : L_X + 3 * 16384u; }
constexpr unsigned L_K = L_X + 4 * 16384u;
#define LANEVARS() \
  int tid = threadIdx.x; asm volatile("" : "+v"(tid)); \
  const int lane = tid & 63, r16 = lane & 15, g = lane >> 4; \
  const unsigned q4 = r16 >> 2, p4 = lane & 3; \
  const unsigned vD = (unsigned)((lane >> 3) * 1024 + n64 * 128 + ((((lane & 7) >> 1) ^ g) * 32) + (lane & 1) * 16); \
  auto fill = [&](int j, int sl) { if (NOBULK) return; \
    _Pragma("unroll") for (int i = 0; i < 4; ++i) glds16(slotoff(sl) + q * 4096 + i * 1024, vD, sD, (unsigned)((11 * q + j) * 32 + 8 * i) * 1024u); }; \
  auto pollflags = [&](unsigned base, int n, unsigned stride) { \
    unsigned a_ = lane < n ? base + lane * stride : OOB; \
    if (stride == 128u) __builtin_amdgcn_s_sleep(PRE_SLEEP); \
    _Pragma("unroll 1") for (unsigned spins = 0; spins < SPIN_CAP; ++spins) { \
      const unsigned f = __builtin_amdgcn_raw_buffer_load_b32(sWS, a_, 0, 16); \
      if (f == tag) a_ = OOB; \
      if (__all(a_ == OOB)) break; \
      __builtin_amdgcn_s_sleep(SLP_FLAG); \
    } \
    asm volatile("" ::: "memory"); \
  };
#define PREAMBLE() \
  const KARG Params* kp = (const KARG Params*)__builtin_amdgcn_kernarg_segment_ptr(); asm volatile("" : "+s"(kp)); \
  const KARG Params& p = *kp; \
  int bid = blockIdx.x, w = w_; asm volatile("" : "+s"(bid), "+s"(w)); \
  const int q = w & 3, xcd = bid & 7, slot = bid >> 3; \
  const srd_t sWS = mk_srd(p.ws, WS_END); \
  const int tb = (xcd >> 2) * 8 + (slot >> 2), cg = (xcd & 3) * 4 + (slot & 3); \
  const int t2 = xcd * 4 + (slot >> 3), n64 = slot & 7; \
  const int wm = q & 1, wn = q >> 1, t64 = tb * 2 + wm; \
  const bool has3 = (cg & 3) < 3 && cg != 14; \
  const int fb0 = 11 * (cg >> 2) + 3 * (cg & 3) - (cg == 15); \
  const srd_t sX = mk_srd(p.x, SEQ * DM * 2); \
  const srd_t sD = mk_srd(p.wd, FF * DM * 2); \
  const int jmat = bid >= 86, jj = bid - 86 * jmat, jkh = jj >= 43, jln = jj - 43 * jkh; \
  unsigned tag; \
  f4 acc2[4][4] = {};
#define FINAL_LOADS() \
  u2 xv[2], stg[2]; \
  _Pragma("unroll") for (int i = 0; i < 2; ++i) { \
    const int tok = t2 * 64 + tokmap((w >> 2) + 2 * i, 4 * q + g); \
    xv[i] = __builtin_amdgcn_raw_buffer_load_b64(sX, (unsigned)(tok * 1024 + n64 * 128 + r16 * 8), 0, 0); \
    stg[i] = __builtin_amdgcn_raw_buffer_load_b64(sWS, WS_STATS + (unsigned)tok * 8u, 0, 16); \
  } \
  const u2 dk = __builtin_amdgcn_raw_buffer_load_b64(mk_srd(p.dsk, DM * 2), (unsigned)(n64 * 128 + r16 * 8), 0, 0);
#define FINAL_PREP() \
  f4 XD[2], XS[2]; \
  _Pragma("unroll") for (int i = 0; i < 2; ++i) { \
    _Pragma("unroll 1") for (unsigned spins = 0; spins < SPIN_CAP && !__all(stg[i].y == tag); ++spins) \
      stg[i] = __builtin_amdgcn_raw_buffer_load_b64(sWS, WS_STATS + (unsigned)(t2 * 64 + tokmap((w >> 2) + 2 * i, 4 * q + g)) * 8u, 0, 16); \
    const float mean = bf_lo(stg[i].x), rs = __builtin_amdgcn_rcpf(bf_hi(stg[i].x)); \
    XS[i] = f4{bf_lo(xv[i].x), bf_hi(xv[i].x), bf_lo(xv[i].y), bf_hi(xv[i].y)}; \
    const float ds[4] = {bf_lo(dk.x), bf_hi(dk.x), bf_lo(dk.y), bf_hi(dk.y)}; \
    _Pragma("unroll") for (int j = 0; j < 4; ++j) XD[i][j] = rbf(rbf(rbf(XS[i][j] - mean) * rs) * ds[j]); \
  } \
  u2 xd[2] = {u2{pk_bf(XD[0][0], XD[0][1]), pk_bf(XD[0][2], XD[0][3])}, u2{pk_bf(XD[1][0], XD[1][1]), pk_bf(XD[1][2], XD[1][3])}}; \
  asm volatile("" : "+v"(xd[0]), "+v"(xd[1]), "+v"(xv[0]), "+v"(xv[1]));
#define FINAL_OUT() \
  _Pragma("unroll") for (int i = 0; i < 2; ++i) { \
    const int r = 4 * q + g; \
    const unsigned o = (unsigned)(r * 256 + ((r16 ^ r) * 16)); \
    LAS char* ss = smem + (w < 4 ? slotoff(2 + 2 * i) : slotoff(3 + 2 * i)) + o; \
    LAS char* sf = smem + (w < 4 ? slotoff(6 + 2 * i) : slotoff(i ? 0 : 7)) + o; \
    const f4 yf = (*(LAS f4*)(sf) + *(LAS f4*)(sf + 4096)) + (*(LAS f4*)(sf + 8192) + *(LAS f4*)(sf + 12288)); \
    const f4 ys = (*(LAS f4*)(ss) + *(LAS f4*)(ss + 4096)) + (*(LAS f4*)(ss + 8192) + *(LAS f4*)(ss + 12288)); \
    f4 G; \
    _Pragma("unroll") for (int j = 0; j < 4; ++j) { const float f = rbf(yf[j]); G[j] = rbf(f * __builtin_amdgcn_rcpf(1.0f + __expf(-f))); } \
    *(f4*)(p.out + (size_t)(t2 * 64 + tokmap((w >> 2) + 2 * i, r)) * DM + n64 * 64 + r16 * 4) = (ys + f4{bf_lo(xd[i].x), bf_hi(xd[i].x), bf_lo(xd[i].y), bf_hi(xd[i].y)}) * G + f4{bf_lo(xv[i].x), bf_hi(xv[i].x), bf_lo(xv[i].y), bf_hi(xv[i].y)}; \
  }
DEVI bf8 cat(bf4 a, bf4 b) { return __builtin_shufflevector(a, b, 0, 1, 2, 3, 4, 5, 6, 7); }
DEVI void lds_wait(LAS char* a, unsigned n) { while (lds_ld(a) < n) __builtin_amdgcn_s_sleep(1); asm volatile("" ::: "memory"); }
DEVI void lds_post(LAS char* a) { asm volatile("s_waitcnt lgkmcnt(0)" ::: "memory"); lds_st(a, 1u); }

__global__ __launch_bounds__(512, 2) void s5_glu_kernel(Params p_) {
  __shared__ __attribute__((aligned(16))) char smem_[LDS_BYTES];
  LAS char* smem = (LAS char*)smem_;
  auto rd128 = [&](unsigned addr) -> bf8 { return *(LAS bf8*)(smem + addr); };
  auto rdtr = [&](unsigned addr) -> bf4 { return __builtin_amdgcn_ds_read_tr16_b64_v4bf16((LAS bf4*)(smem + addr)); };
  asm volatile(".Ls5_start:");
  STAMP(0);
  const int w_ = __builtin_amdgcn_readfirstlane(threadIdx.x >> 6);
  if (w_ < 4) {
    PREAMBLE()
    f4 ag[4][4] = {}, av[2][4] = {};
    {
      LANEVARS()
      const int cn64 = bid & 7, cks = bid >> 3;
      u4 wj[4];
      {
        const int brow = (tb >> 3) * 512 + cg * 32 + (tb & 7) * 4 + q, xr = tb * 128 + cg * 8 + 2 * q;
        const srd_t sBB = mk_srd((brow >> 9) ? p.bim : p.bre, DM * DM * 2);
        u4 xa[2];
#pragma unroll
        for (int i = 0; i < 2; ++i) xa[i] = __builtin_amdgcn_raw_buffer_load_b128(sX, lane * 16 + i * 1024, (unsigned)xr * 1024u, 0);
        const u4 br = __builtin_amdgcn_raw_buffer_load_b128(sBB, lane * 16, (unsigned)(brow & 511) * 1024u, 0);
        const unsigned ep = __builtin_amdgcn_raw_buffer_load_b32(sWS, 0, WS_EPOCH, 16);
        const unsigned ch2 = (unsigned)(cg * 32 + wn * 16 + r16) * 2u;
        const unsigned short pa = __builtin_amdgcn_raw_buffer_load_b16(mk_srd(p.lre, 1024), ch2, 0, 0), pb = __builtin_amdgcn_raw_buffer_load_b16(mk_srd(p.lim, 1024), ch2, 0, 0),
                             pc = __builtin_amdgcn_raw_buffer_load_b16(mk_srd(p.ldt, 1024), ch2, 0, 0);
        const u4 ct = __builtin_amdgcn_raw_buffer_load_b128(mk_srd((cks & 1) ? p.cim : p.cre, DM * DM * 2), r16 * 1024 + g * 16, (unsigned)((cn64 * 64 + q * 16) * 1024 + (cks >> 1) * 64), 0);
        auto late_loads = [&]() {
          const srd_t sJ = mk_srd(jmat ? p.wu : p.wg, bid < 172 ? DM * FF * 2 : 0);
          const unsigned vj = (unsigned)((lane >> 3) * 5504 + (lane & 7) * 16), sj = (unsigned)((jkh * 128 + q * 32) * 5504 + jln * 128);
#pragma unroll
          for (int i = 0; i < 4; ++i) wj[i] = __builtin_amdgcn_raw_buffer_load_b128(sJ, vj, sj + i * 8 * 5504, 0);
        };
        if (LATE == 0) late_loads();
        SB0();
        STAMP(1);
        float mean[2], sd[2];
#pragma unroll
        for (int i = 0; i < 2; ++i) {
          float v[8];
#pragma unroll
          for (int j = 0; j < 4; ++j) { v[2 * j] = bf_lo(xa[i][j]); v[2 * j + 1] = bf_hi(xa[i][j]); }
          if (LATE == 1 && i == 0) { asm volatile("" : "+v"(v[0]) :: "memory"); late_loads(); asm volatile("" ::: "memory"); }
          mean[i] = wave_sum(((v[0] + v[1]) + (v[2] + v[3])) + ((v[4] + v[5]) + (v[6] + v[7]))) * (1.0f / DM);
          float qq = 0.f;
#pragma unroll
          for (int j = 0; j < 8; ++j) { const float dd = v[j] - mean[i]; qq = fmaf(dd, dd, qq); }
          sd[i] = __builtin_amdgcn_sqrtf(rbf(rbf(wave_sum(qq) * (1.0f / DM)) + 1e-5f));
        }
        tag = __builtin_amdgcn_readfirstlane(ep) + 1u;
        if (tid < 32) lds_st(smem + L_CNT + tid * 4, tid == 1 ? tag : 0u);
        st128(u4{pk_bf(mean[0], sd[0]), tag, pk_bf(mean[1], sd[1]), tag}, sWS, lane == 0 ? WS_STATS + (unsigned)xr * 8u : OOB, 0);
        float s = 0.f;
#pragma unroll
        for (int j = 0; j < 4; ++j) s += bf_lo(br[j]) + bf_hi(br[j]);
        s = wave_sum(s);
        __builtin_amdgcn_raw_buffer_store_b64(u2{__float_as_uint(s), tag}, sWS, lane == 0 ? WS_CSB + (unsigned)brow * 8u : OOB, 0, 16);
        st128(ct, sWS, lane * 16, WS_CT + (unsigned)((cn64 * 32 + cks) * 4 + q) * 1024u);
        if (LATE == 2) { asm volatile("" ::: "memory"); late_loads(); }
        SB0();
        if (wm == 0) {
          const Ssm K = ssm_consts(pa, pb, pc);
          const cf l32 = csq(K.l16), l48 = cmul(l32, K.l16), l128 = csq(K.l64), l256 = csq(l128), l384 = cmul(l256, l128);
          const cf l512 = csq(l256), pwg = sel4(g, cf{1.f, 0.f}, l128, l256, l384);
          const cf S0 = cmul(K.coef2, sel4(g, cf{1.f, 0.f}, K.l16, l32, l48)), S1 = cmul(K.l8, S0);
          LAS char* kt = smem + L_K + wn * 8192 + lane * 16;
          *(LAS f4*)(kt) = f4{K.lam.r, K.lam.i, K.coef2.r, K.coef2.i}; *(LAS f4*)(kt + 1024) = f4{K.l8.r, K.l8.i, K.l16.r, K.l16.i};
          *(LAS f4*)(kt + 2048) = f4{K.l64.r, K.l64.i, l512.r, l512.i}; *(LAS f4*)(kt + 3072) = f4{S0.r, S0.i, S1.r, S1.i};
          const cf wT = sel4(g, l48, l32, K.l16, cf{1.f, 0.f});
          *(LAS f4*)(kt + 4096) = f4{pwg.r, pwg.i, wT.r, wT.i};
        }
      }
      auto late_jobs = [&]() {
        u4 sel;
#pragma unroll
        for (int i = 0; i < 4; ++i) sel[i] = (r16 >> 1) == i ? ((r16 & 1) ? 0x3f800000u : 0x00003f80u) : 0u;
        f4 d = {0.f, 0.f, 0.f, 0.f};
#pragma unroll
        for (int i = 0; i < 4; ++i) d = mfma(asb(wj[i]), asb(sel), d);
#pragma unroll
        for (int j = 0; j < 4; ++j) d[j] = hsum(d[j]);
        if ((lane & 0x28) == 0) *(LAS f4*)(smem + L_TR + q * 256 + (g * 8 + r16) * 16) = d;
      };
      unsigned ax[4], aw[4];
      const unsigned aA0 = (unsigned)(r16 * 128 + ((g ^ (r16 >> 1)) * 16));
      ax[0] = (aA0 + wm * 8192) ^ (wn * 4096); ax[1] = ax[0] ^ 64u; ax[2] = ax[0] ^ 4096u; ax[3] = ax[2] ^ 64u;
      aw[2] = (unsigned)((8 * g + q4) * 192 + 8 * p4 + (g & 1) * 32) + 128u; aw[3] = aw[2] ^ 32u;
      aw[0] = aw[2] - 128u + wn * 64u; aw[1] = aw[0] ^ 32u;
#pragma unroll
      for (int i = 0; i < 4; ++i) { aw[i] += L_W; asm volatile("" : "+v"(aw[i]), "+v"(ax[i])); }
      bf8 fa[2][4], fw[4], fv[2][4];
      auto wfrag = [&](int b, int i, int slab) -> bf8 {
        const unsigned a = aw[b + (i & 1)] + (i >> 1) * 6144 + (wslab(slab) - (slab < 13 ? L_W : slab < 15 ? L_SB : wslab(15)));
        return cat(rdtr(a), rdtr(a + 768));
      };
      auto step = [&](auto H_) {
        constexpr int H = decltype(H_)::v, c = H & 1, n = c ^ 1;
        constexpr int kk = (H + 1) & 1;
        if (H == 2) late_jobs();
        if (H > 0 && H < 10) { waitv<(H == 1 ? 4 : 63)>(); BAR(); }
        if (H == 11) lds_wait(smem + L_CNT + 8, 4u);
        if (H == 11 || H == 12 || H == 15) { waitv<63>(); if (lane == 0) __hip_atomic_fetch_add((LAS unsigned*)(smem + L_CNT + 20 + H * 4), 1u, __ATOMIC_RELAXED, __HIP_MEMORY_SCOPE_WORKGROUP); }
        if (H == 1) __builtin_amdgcn_raw_buffer_store_b32(tag, sWS, lane == 0 ? WS_FLC + (unsigned)((cn64 * 32 + cks) * 4 + q) * 4u : OOB, 0, 16);
        if (H == 12 || H == 13) lds_wait(smem + L_CNT + 16 + H * 4, 4u);
        SB0();
#pragma unroll
        for (int i = 0; i < 24; ++i) {
          if ((H == 9 || H >= 12) && i % 6 == 3) { constexpr int sl = H == 9 ? 0 : H - 11; glds16(slotoff(sl) + q * 4096 + (i / 6) * 1024, vD, sD, (unsigned)((11 * q + sl) * 32 + 8 * (i / 6)) * 1024u); }
          if (i < 16) ag[i & 3][i >> 2] = mfma(fw[i >> 2], fa[c][i & 3], ag[i & 3][i >> 2]);
          else av[i & 1][(i - 16) >> 1] = mfma(fv[c][(i - 16) >> 1], fa[c][i & 1], av[i & 1][(i - 16) >> 1]);
          if (H < 15) {
            if ((i & 3) == 1 && i < 16) fa[n][i >> 2] = rd128(ax[((i >> 2) & 2) + kk] + ((i >> 2) & 1) * 2048);
            else if ((i & 3) == 2 && i < 16) fv[n][i >> 2] = wfrag(2, i >> 2, H + 1);
            else if ((i & 3) == 0 && i >= 4 && i <= 16) fw[(i >> 2) - 1] = wfrag(0, (i >> 2) - 1, H + 1);
            if (kk == 1 && H < 14 && i >= 16 && i < 20) { ax[i - 16] += ((H >> 1) % 5 == 4) ? -4 * 16384 : 16384; asm volatile("" : "+v"(ax[i - 16])); }
            if ((H == 11 || H == 13) && i >= 20) { aw[i - 20] += H == 11 ? L_SB - L_W : wslab(15) - L_SB; asm volatile("" : "+v"(aw[i - 20])); }
          }
          SB0();
        }
      };
      STAMP(2);
      waitv<63>(); BAR();
      STAMP(3);
#pragma unroll
      for (int i = 0; i < 4; ++i) { fw[i] = wfrag(0, i, 0); fa[0][i] = rd128(ax[i & 2] + (i & 1) * 2048); }
#pragma unroll
      for (int i = 0; i < 4; ++i) fv[0][i] = wfrag(2, i, 0);
      step(IC<0>{}); step(IC<1>{}); step(IC<2>{}); step(IC<3>{}); step(IC<4>{}); step(IC<5>{}); step(IC<6>{}); step(IC<7>{});
      STAMP(4);
      step(IC<8>{}); FSTAMP(1); step(IC<9>{}); FSTAMP(2); step(IC<10>{}); FSTAMP(14); step(IC<11>{}); FSTAMP(15); step(IC<12>{}); step(IC<13>{}); step(IC<14>{}); step(IC<15>{});
    }
    STAMP(5);
    LANEVARS()
    lds_wait(smem + L_CNT, 2u);
    {
      auto glu_frag = [&](const f4 (&ac)[4], const f4 (&cs)[4], f2 st) -> u4 {
        const f2 nm = sp(opq(st.y)), a2 = sp(st.x);
        unsigned o[4];
#pragma unroll
        for (int i = 0; i < 2; ++i) {
          f2 g0 = fma2(nm, lo2(cs[i]), lo2(ac[i])), g1 = fma2(nm, hi2(cs[i]), hi2(ac[i]));
          f2 u0 = fma2(nm, lo2(cs[2 + i]), lo2(ac[2 + i])) * a2, u1 = fma2(nm, hi2(cs[2 + i]), hi2(ac[2 + i])) * a2;
          g0 = f2{fmaxf(g0.x, 0.f), fmaxf(g0.y, 0.f)} * u0;
          g1 = f2{fmaxf(g1.x, 0.f), fmaxf(g1.y, 0.f)} * u1;
          o[2 * i] = pk_bf(g0.x, g0.y); o[2 * i + 1] = pk_bf(g1.x, g1.y);
        }
        return u4{o[0], o[1], o[2], o[3]};
      };
      f4 cs[4], cv[4]; f2 st[4];
#pragma unroll
      for (int i = 0; i < 4; ++i) {
        cs[i] = *(LAS f4*)(smem + L_CS + ((i >> 1) * 96 + wn * 32 + (i & 1) * 16 + g * 4) * 4);
        st[i] = *(LAS f2*)(smem + L_PR + (wm * 64 + r16) * 8 + ((i * 128) ^ (wn * 256)));
      }
#pragma unroll
      for (int i = 0; i < 4; ++i) cv[i] = *(LAS f4*)(smem + L_CS + ((i >> 1) * 96 + 64 + (i & 1) * 16 + g * 4) * 4);
      const unsigned so = WS_HF + (unsigned)(t64 * 43 + fb0) * 4096u;
      const unsigned vo = (unsigned)(lane * 16 + wn * 2048);
#pragma unroll
      for (int m = 0; m < 4; ++m) { st128(glu_frag(ag[m], cs, st[m]), sWS, (m < 2 ? vo : vo ^ 2048u) + (m & 1) * 1024, so + wn * 4096); SB0(); }
#pragma unroll
      for (int m = 0; m < 2; ++m) { st128(glu_frag(av[m], cv, st[m]), sWS, vo + m * 1024, has3 ? so + 8192 : OOB); SB0(); }
    }
    STAMP(6);
    const unsigned aD = q * 4096 + (4 * g + q4) * 128 + (((2 * g + (q4 >> 1)) & 3) * 32) + p4 * 8;
    auto bfrag = [&](int sl, int nt) -> bf8 { return cat(rdtr((aD ^ (nt * 32)) + slotoff(sl)), rdtr((aD ^ (nt * 32)) + slotoff(sl) + 2048)); };
    asm volatile("s_waitcnt vmcnt(0)" ::: "memory");
    __builtin_amdgcn_raw_buffer_store_b32(tag, sWS, lane == 0 ? WS_FLF + (unsigned)(t64 * 32 + cg * 2 + wn) * 128u : OOB, 0, 16);
    STAMP(7);
    lds_wait(smem + L_CNT + 80, 4u);
    FINAL_LOADS()
    fill(5, 5); fill(6, 6); fill(7, 7); fill(8, 8);
    FINAL_PREP()
    pollflags(WS_FLF + (unsigned)(t2 * 32 + q * 8) * 128u, 8, 128u);
    STAMP(8);
    u4 af[11][4];
    auto lda = [&](int j, int m) {
      af[j][m] = __builtin_amdgcn_raw_buffer_load_b128(sWS, lane * 16 + m * 1024, (j < 10 || q < 3) ? WS_HF + (unsigned)(t2 * 43 + 11 * q + j) * 4096u : OOB, 16);
    };
#pragma unroll
    for (int n = 0; n < WIN0 * 4; ++n) lda(n >> 2, n & 3);
    STAMP(9);
    SB0();
    {
      bf8 bc[4], bn[4];
      const unsigned pw = q * 4096 + r16 * 256 + ((g ^ r16) * 16);
#pragma unroll
      for (int nt = 0; nt < 4; ++nt) bc[nt] = bfrag(0, nt);
#pragma unroll
      for (int j = 0; j < 11; ++j) {
#pragma unroll
        for (int i = 0; i < 16; ++i) {
          acc2[i >> 2][i & 3] = mfma(bc[i >> 2], asb(af[j][i & 3]), acc2[i >> 2][i & 3]);
          if (j + 1 < 11 && i < 4) bn[i] = bfrag(j + 1 < 9 ? j + 1 : j + 1 - 9, i);
          {
            constexpr int R = WINF - WIN0;
            if (j < R) { if (i & 1) { const int n = (WIN0 + 2 * j) * 4 + (i >> 1); lda(n >> 2, n & 3); } }
            else if (j + WINF < 11 && (i & 3) == 3) lda(j + WINF, i >> 2);
          }
          if (j >= 1 && j <= 2 && (i & 3) == 2) glds16(slotoff(j - 1) + q * 4096 + (i >> 2) * 1024, vD, sD, (unsigned)((11 * q + 8 + j) * 32 + 8 * (i >> 2)) * 1024u);
          if (j == 4 && i == 12) lds_post(smem + L_CNT + 16 + q * 4);
          if (j == 10 && i >= 4) { const int k = i - 4; *(LAS f4*)(smem + slotoff((k & 3) < 3 ? 6 + (k & 3) : 0) + (pw ^ ((k >> 2) * 64))) = acc2[k >> 2][k & 3]; }
          SB0();
        }
#pragma unroll
        for (int nt = 0; nt < 4; ++nt) bc[nt] = bn[nt];
      }
      STAMP(10);
#pragma unroll
      for (int k = 12; k < 16; ++k) *(LAS f4*)(smem + slotoff((k & 3) < 3 ? 6 + (k & 3) : 0) + (pw ^ ((k >> 2) * 64))) = acc2[k >> 2][k & 3];
    }
    waitv<63>();
    STAMP(11);
    BAR();
    STAMP(12);
    FINAL_OUT()
    STAMP(13);
    STAMP_OUT();
    asm volatile(".Ls5_end_a:");
    __builtin_amdgcn_endpgm();
  } else {
    PREAMBLE()
    LANEVARS()
    {
      unsigned long long pc; unsigned len, back;
      asm volatile("s_getpc_b64 %0\n.Ls5_pf_here:\n\ts_mov_b32 %1, .Ls5_end_a-.Ls5_start\n\ts_max_i32 %1, %1, .Ls5_end_b-.Ls5_start\n\ts_mov_b32 %2, .Ls5_pf_here-.Ls5_start" : "=s"(pc), "=s"(len), "=s"(back)::"scc");
      const srd_t sI = mk_srd((const void*)(pc - back), (PFC && q == 0) ? len : 0u);
      glds4(L_JUNK, (unsigned)((lane * 32 + slot) * 128), sI, 0);
    }
    const srd_t sB = mk_srd((q & 1) ? p.bim : p.bre, DM * DM * 2);
    const srd_t sW = mk_srd(q < 2 ? p.wg : p.wu, DM * FF * 2);
    if (PF01) glds4(L_JUNK, lane < 6 ? (unsigned)((bid * 2 + (q & 1)) * 6 + lane) * 128u : OOB, sW, 0);
    unsigned vA[2], vB[2], vW[3];
#pragma unroll
    for (int i = 0; i < 2; ++i) {
      const int r = 8 * i + (lane >> 3);
      const unsigned sw = (unsigned)(((lane & 7) ^ ((r >> 1) & 7)) * 16);
      vA[i] = (unsigned)(tb * 128 + (q >> 1) * 64 + tokmap(2 * (q & 1), r)) * 1024u + sw;
      vB[i] = (unsigned)(cg * 32 + (q >> 1) * 16 + r) * 1024u + sw;
    }
#pragma unroll
    for (int s = 0; s < 3; ++s) {
      const int idx = 64 * (3 * (q & 1) + s) + lane;
      const int k = idx / 12, c = idx - 12 * k;
      const int u = (c >> 1) ^ ((k >> 3) & 1);
      vW[s] = (unsigned)(k * (FF * 2) + (fb0 * 32 + 16 * u + 8 * (c & 1)) * 2);
      if (!has3 && u >= 4) vW[s] = OOB;
    }
    const int rot1 = ROT1 == 1 ? (tb + 2 * cg) & 7 : ROT1 == 2 ? (tb + cg) & 1 : ROT1 == 3 ? (tb + cg) & 3 : 0;
    u4 cfr[8][4];
    auto pc1 = [&](int kind, int s, int i) {
      if (kind == 0) glds16(L_X + (s % 5) * 16384 + q * 4096 + i * 1024, vA[i & 1], sX, 128u * ((s + rot1) & 7) + (i >> 1) * 2048u);
      if (kind == 1) glds16(L_SB + (s % 3) * 8192 + q * 2048 + i * 1024, vB[i], sB, 128u * ((s + rot1) & 7));
      if (kind == 2) glds16(wslab(s) + q * 3072 + i * 1024, vW[i], sW, 32u * FF * 2 * ((s + 2 * rot1) & 15));
      if (kind == 3) cfr[s][i] = __builtin_amdgcn_raw_buffer_load_b128(sWS, lane * 16 + i * 1024, WS_CT + (unsigned)(n64 * 32 + q * 8 + s) * 4096u, 16);
    };
    auto ldX = [&](int s) { pc1(0, s, 0); pc1(0, s, 1); pc1(0, s, 2); pc1(0, s, 3); };
    auto ldB = [&](int s) { pc1(1, s, 0); pc1(1, s, 1); };
    auto ldW = [&](int s) { pc1(2, s, 0); pc1(2, s, 1); pc1(2, s, 2); };
    ldX(0); ldB(0); ldW(0); ldW(1);
    STAMP(1);
    ldX(1);
    asm volatile("s_waitcnt vmcnt(4)" ::: "memory");
    STAMP(2);
    BAR();
    __builtin_amdgcn_s_setprio(LPRIO0);
    STAMP(3);
    const unsigned ch = (unsigned)(cg * 32 + wn * 16 + r16);
    const unsigned aA0 = (unsigned)(r16 * 128 + ((g ^ (r16 >> 1)) * 16));
    tag = __builtin_amdgcn_readfirstlane(lds_ld(smem + L_CNT + 4));
    auto poll8 = [&](unsigned addr, bool need) -> u2 {
      u2 gr = {0u, 0u};
#pragma unroll 1
      for (unsigned spins = 0; spins < SPIN_CAP; ++spins) {
        gr = __builtin_amdgcn_raw_buffer_load_b64(sWS, addr, 0, 16);
        asm volatile("" : "+v"(gr));
        if (__all(!need | (gr.y == tag))) break;
        __builtin_amdgcn_s_sleep(SLP_FLAG);
      }
      return gr;
    };
    f4 as[2][4] = {};
    bf8 ra[2][2][4], rb[2][2][2];
    auto s1_read = [&](int s) {
#pragma unroll
      for (int kk = 0; kk < 2; ++kk) {
#pragma unroll
        for (int i = 0; i < 2; ++i) rb[s & 1][kk][i] = rd128(L_SB + (s % 3) * 8192 + wn * 4096 + i * 2048 + (aA0 ^ (kk * 64)));
#pragma unroll
        for (int m = 0; m < 4; ++m) ra[s & 1][kk][m] = rd128(L_X + (s % 5) * 16384 + wm * 8192 + m * 2048 + (aA0 ^ (kk * 64)));
      }
      SB0();
    };
    auto s1_mma = [&](int s, int k0, int s0, int k1, int s1, int k2, int s2, int k3, int s3) {
      const int kind[4] = {k0, k1, k2, k3}, slab[4] = {s0, s1, s2, s3};
      int gi_ = 0, pi_ = 0;
#pragma unroll
      for (int i = 0; i < 16; ++i) {
        if (gi_ < 4 && kind[gi_] >= 0) { pc1(kind[gi_], slab[gi_], pi_); if (++pi_ == (kind[gi_] == 1 ? 2 : kind[gi_] == 2 ? 3 : 4)) { pi_ = 0; ++gi_; } }
        as[(i >> 2) & 1][i & 3] = mfma(ra[s & 1][i >> 3][i & 3], rb[s & 1][i >> 3][(i >> 2) & 1], as[(i >> 2) & 1][i & 3]);
        SB0();
      }
    };
    auto ldC = [&](int j) {
#pragma unroll
      for (int nt = 0; nt < 4; ++nt) cfr[j][nt] = __builtin_amdgcn_raw_buffer_load_b128(sWS, NOBULK ? OOB : lane * 16 + nt * 1024, WS_CT + (unsigned)(n64 * 32 + q * 8 + j) * 4096u, 16);
    };
#define NEXT(n) do { waitv<n>(); BAR(); } while (0)
    Ssm K; cf l512, pwg, S0, S1, wT;
    s1_read(0);
    {
      LAS char* kt = smem + L_K + wn * 8192 + lane * 16;
      const f4 k0 = *(LAS f4*)(kt), k1 = *(LAS f4*)(kt + 1024), k2 = *(LAS f4*)(kt + 2048), k3 = *(LAS f4*)(kt + 3072);
      const f4 k4 = *(LAS f4*)(kt + 4096);
      ldB(1); ldW(2); ldX(2); ldB(2); ldW(3); ldX(3);
      K.lam = {k0[0], opq(k0[1])}; K.coef2 = {k0[2], opq(k0[3])}; K.l8 = {k1[0], k1[1]}; K.l16 = {k1[2], k1[3]}; K.l64 = {k2[0], k2[1]};
      l512 = {k2[2], k2[3]}; S0 = {k3[0], k3[1]}; S1 = {k3[2], k3[3]}; pwg = {k4[0], k4[1]}; wT = {k4[2], k4[3]};
    }
    NEXT(13);
    s1_read(1); s1_mma(0, 1, 3, 2, 4, 2, 5, 0, 4); NEXT(16);
    s1_read(2);
    {
      float s = 0.f;
#pragma unroll
      for (int i = 0; i < 4; ++i) s += *(LAS float*)(smem + L_TR + i * 256 + ((lane >> 5) * 8 + (lane & 7)) * 16 + ((lane >> 3) & 3) * 4);
      s1_mma(1, 1, 4, 0, 5, 2, 6, -1, 0);
      __builtin_amdgcn_raw_buffer_store_b64(u2{__float_as_uint(s), tag}, sWS, lane * 8, (q == 3 && bid < 172) ? WS_CSW + (unsigned)((jmat * 2 + jkh) * 2752 + jln * 64) * 8u : OOB, 16);
    }
    NEXT(17);
    s1_read(3); s1_mma(2, 1, 5, 0, 6, 2, 7, -1, 0);
    if (PFW) glds4(L_JUNK, lane < 43 ? (unsigned)((slot * 8 + q) * 43 + lane) * 128u : OOB, sD, 0);
    else glds4(L_JUNK, lane < 11 ? (unsigned)((bid * 4 + q) * 11 + lane) * 128u : OOB, sD, 0);
    NEXT(18);
    unsigned fl;
    asm volatile("buffer_load_dword %0, %1, %2, 0 offen sc1" : "=&v"(fl) : "v"(WS_FLC + (unsigned)(n64 * 512 + q * 128 + (lane & 31) * 4)), "s"(sWS) : "memory");
    s1_read(4); s1_mma(3, 1, 6, 2, 8, -1, 0, -1, 0);
    if (PFW) glds4(L_JUNK, lane < 43 ? (unsigned)((slot * 8 + 4 + q) * 43 + lane) * 128u : OOB, sD, 0);
    NEXT(14);
    u2 gr;
    const unsigned ga = q < 2 ? WS_STATS + (unsigned)(tb * 128 + q * 64 + tokmap(g, r16)) * 8u : WS_CSB + (unsigned)((lane >> 5) * 512 + cg * 32 + (lane & 31)) * 8u;
    s1_read(5); s1_mma(4, 1, 7, 0, 7, 2, 9, -1, 0);
    asm volatile("buffer_load_dwordx2 %0, %1, %2, 0 offen sc1" : "=&v"(gr) : "v"(ga), "s"(sWS) : "memory");
    asm volatile("s_waitcnt vmcnt(13)" : "+v"(fl)::"memory");
    if (__builtin_expect(!__all(fl == tag), 0)) pollflags(WS_FLC + (unsigned)(n64 * 512 + q * 128), 32, 4u);
    NEXT(13);
    if (PFW) glds4_sc1(L_JUNK, lane < 32 ? lane * 128u : OOB, sWS, WS_CT + (unsigned)(n64 * 32 + q * 8 + 4 + (t2 & 3)) * 4096u);
    s1_read(6); s1_mma(5, 2, 10, CP, 0, CP, 1, -1, 0);
    asm volatile("s_waitcnt vmcnt(%1)" : "+v"(gr) : "i"(CP > 0 ? 11 : 3) : "memory");
    if (__builtin_expect(!__all(gr.y == tag), 0)) gr = poll8(ga, true);
    if (q < 2) {
      const float rs = __builtin_amdgcn_rcpf(bf_hi(gr.x)), nm = -bf_lo(gr.x);
      *(LAS float*)(smem + L_RS + (q * 64 + lane) * 4) = rs;
      *(LAS float*)(smem + L_NM + (q * 64 + lane) * 4) = rs * nm;
      *(LAS f2*)(smem + L_PR + (q * 64 + lane) * 8) = f2{rs * rs, nm};
    } else *(LAS unsigned*)(smem + L_CSB + lane * 4) = gr.x;
    NEXT(63);
    f4 rs[4], br[4], bi[4];
    s1_read(7);
    {
      const float csr = *(LAS float*)(smem + L_CSB + (wn * 16 + r16) * 4), csi = opq(*(LAS float*)(smem + L_CSB + (32 + wn * 16 + r16) * 4));
#pragma unroll
      for (int m = 0; m < 4; ++m) br[m] = *(LAS f4*)(smem + L_NM + (wm * 64 + 16 * m + 4 * g) * 4);
      SB0();
      s1_mma(6, 2, 11, 2, 15, CP, 2, -1, 0);
#pragma unroll
      for (int m = 0; m < 4; ++m) { bi[m] = br[m] * csi; br[m] = br[m] * csr; asm volatile("" : "+v"(br[m]), "+v"(bi[m])); }
    }
    NEXT(63);
    STAMP(4);
#pragma unroll
    for (int m = 0; m < 4; ++m) rs[m] = *(LAS f4*)(smem + L_RS + (wm * 64 + 16 * m + 4 * g) * 4);
    u4 gw[4];
    const unsigned gb = (wm == 0 && lane < (has3 ? 48 : 32)) ? WS_CSW + (unsigned)(wn * 5504 + fb0 * 32 + 2 * lane) * 8u : OOB;
    asm volatile("buffer_load_dwordx4 %0, %4, %5, 0 offen sc1\n\tbuffer_load_dwordx4 %1, %4, %5, %6 offen sc1\n\tbuffer_load_dwordx4 %2, %4, %5, %7 offen sc1\n\tbuffer_load_dwordx4 %3, %4, %5, %8 offen sc1"
                 : "=&v"(gw[0]), "=&v"(gw[1]), "=&v"(gw[2]), "=&v"(gw[3]) : "v"(gb), "s"(sWS), "s"(1376 * 8), "s"(2 * 1376 * 8), "s"(3 * 1376 * 8) : "memory");
    SB0();
    s1_mma(7, 2, 12, 2, 13, 2, 14, CP, 3);
    FSTAMP(1);
    NEXT((CP > 0 ? 24 : 16));
    FSTAMP(2);
    f2 vr[8], vi[8];
    const f2 lr2 = sp(K.lam.r), li2 = sp(K.lam.i);
#pragma unroll
    for (int m = 0; m < 4; ++m) {
      vr[2 * m] = fma2(lo2(rs[m]), lo2(as[0][m]), lo2(br[m])); vr[2 * m + 1] = fma2(hi2(rs[m]), hi2(as[0][m]), hi2(br[m]));
      vi[2 * m] = fma2(lo2(rs[m]), lo2(as[1][m]), lo2(bi[m])); vi[2 * m + 1] = fma2(hi2(rs[m]), hi2(as[1][m]), hi2(bi[m]));
    }
    f2 hr = vr[0], hi = vi[0];
#pragma unroll
    for (int k = 1; k < 8; ++k) {
      const f2 nr = fma2(lr2, hr, fma2(-li2, hi, vr[k])), ni = fma2(lr2, hi, fma2(li2, hr, vi[k]));
      hr = nr; hi = ni;
    }
    const cf runA = {hr.x, hi.x};
    const cf agg = cfma(K.l8, runA, cf{hr.y, hi.y});
    cf T = cmul(wT, agg);
    T = {gsum(T.r), gsum(T.i)};
    LAS char* car = smem + L_CAR + wn * 256 + r16 * 8;
    asm volatile("" : "+v"(T.r), "+v"(T.i));
    FSTAMP(14);
    if (wm == 0) {
      *(LAS f2*)car = f2{T.r, T.i};
      lds_post(smem + L_CNT + 48 + wn * 4);
      asm volatile("s_waitcnt vmcnt(%4)" : "+v"(gw[0]), "+v"(gw[1]), "+v"(gw[2]), "+v"(gw[3]) : "i"(CP > 0 ? 4 : 0) : "memory");
      if (lane == 0) __hip_atomic_fetch_add((LAS unsigned*)(smem + L_CNT + 8), 1u, __ATOMIC_RELAXED, __HIP_MEMORY_SCOPE_WORKGROUP);
#pragma unroll 1
      for (unsigned spins = 0; spins < SPIN_CAP; ++spins) {
        bool ok = true;
#pragma unroll
        for (int i = 0; i < 4; ++i) ok &= (gw[i].y == tag) & (gw[i].w == tag);
        if (__builtin_expect(__all(ok | (gb == OOB)), 1)) break;
        __builtin_amdgcn_s_sleep(SLP_FLAG);
#pragma unroll
        for (int i = 0; i < 4; ++i) gw[i] = __builtin_amdgcn_raw_buffer_load_b128(sWS, gb, i * 1376 * 8, 16);
      }
      if (lane < 48) *(LAS f2*)(smem + L_CS + wn * 384 + lane * 8) = f2{(__uint_as_float(gw[0].x) + __uint_as_float(gw[1].x)) + (__uint_as_float(gw[2].x) + __uint_as_float(gw[3].x)),
                                                                      (__uint_as_float(gw[0].z) + __uint_as_float(gw[1].z)) + (__uint_as_float(gw[2].z) + __uint_as_float(gw[3].z))};
      waitv<63>();
      if (lane == 0) __hip_atomic_fetch_add((LAS unsigned*)(smem + L_CNT), 1u, __ATOMIC_RELAXED, __HIP_MEMORY_SCOPE_WORKGROUP);
    } else {
      lds_wait(smem + L_CNT + 48 + wn * 4, 1u);
      const f2 t0 = *(LAS f2*)car;
      const cf W = cfma(K.l64, cf{t0.x, opq(t0.y)}, T);
      st128(u4{__float_as_uint(W.r), tag, __float_as_uint(W.i), tag}, sWS, g == 0 ? ch * 16u : OOB, WS_AGG + (unsigned)tb * 8192u);
      FSTAMP(15);
      asm volatile("s_waitcnt vmcnt(%4)" : "+v"(gw[0]), "+v"(gw[1]), "+v"(gw[2]), "+v"(gw[3]) : "i"(CP > 0 ? 5 : 1) : "memory");
      if (lane == 0) __hip_atomic_fetch_add((LAS unsigned*)(smem + L_CNT + 8), 1u, __ATOMIC_RELAXED, __HIP_MEMORY_SCOPE_WORKGROUP);
    }
    if (!KEEPPRIO) __builtin_amdgcn_s_setprio(0);
    STAMP(5);
    u4 Xg[4]; unsigned ad[4];
#pragma unroll
    for (int i = 0; i < 4; ++i) {
      const int src = tb - 1 - (4 * i + g);
      const unsigned a0 = src >= 0 ? WS_AGG + (unsigned)src * 8192u + ch * 16u : OOB;
      ad[i] = wm ? OOB : a0;
    }
    const f2 cr2 = sp(K.coef2.r), ci2 = sp(K.coef2.i);
#pragma unroll
    for (int k = 0; k < 8; ++k) {
      if (CP > 0) { if (!(k & 1)) ldC(4 + (k >> 1)); } else if (k < NC_MID) ldC(k);
      if (k == SWEEP_AT) {
#pragma unroll
        for (int i = 0; i < 4; ++i) Xg[i] = __builtin_amdgcn_raw_buffer_load_b128(sWS, ad[i], 0, 16);
      }
      const f2 a = fma2(cr2, vr[k], -(ci2 * vi[k])), b = -fma2(cr2, vi[k], ci2 * vr[k]);
      vr[k] = a; vi[k] = b;
      asm volatile("" : "+v"(vr[k]), "+v"(vi[k])::"memory");
    }
    if (SWEEP_AT == 8) {
#pragma unroll
      for (int i = 0; i < 4; ++i) Xg[i] = __builtin_amdgcn_raw_buffer_load_b128(sWS, ad[i], 0, 16);
      asm volatile("" ::: "memory");
    }
    cf E0, E1;
    {
      cf A0, A1, A2, A3;
      gather4(agg.r, A0.r, A1.r, A2.r, A3.r);
      gather4(agg.i, A0.i, A1.i, A2.i, A3.i);
      const cf P2 = cfma(K.l16, A0, A1), P3 = cfma(K.l16, P2, A2);
      E0 = cmul(K.coef2, sel4(g, cf{0.f, 0.f}, A0, P2, P3)); E1 = cfma(K.l8, E0, cmul(K.coef2, runA));
      asm volatile("" : "+v"(E0.r), "+v"(E0.i), "+v"(E1.r), "+v"(E1.i));
    }
    STAMP(6);
#pragma unroll 1
    for (unsigned spins = 0; spins < SPIN_CAP; ++spins) {
      unsigned todo = 0;
#pragma unroll
      for (int i = 0; i < 4; ++i) todo |= __all((ad[i] == OOB) | ((Xg[i].y == tag) & (Xg[i].w == tag))) ? 0u : 1u << i;
      if (!todo) break;
      __builtin_amdgcn_s_sleep(SLP_SWEEP);
#pragma unroll
      for (int i = 0; i < 4; ++i) if (todo >> i & 1) Xg[i] = __builtin_amdgcn_raw_buffer_load_b128(sWS, ad[i], 0, 16);
    }
    cf carry;
    if (wm == 0) {
      cf c = {__uint_as_float(Xg[3].x), __uint_as_float(Xg[3].z)};
#pragma unroll
      for (int i = 2; i >= 0; --i) c = cfma(l512, c, cf{__uint_as_float(Xg[i].x), __uint_as_float(Xg[i].z)});
      c = cmul(c, pwg);
      carry = {gsum(c.r), gsum(c.i)};
      const cf c1 = cfma(K.l64, carry, T);
      *(LAS f2*)(car + 128) = f2{c1.r, c1.i};
      lds_post(smem + L_CNT + 56 + wn * 4);
      if (tb == 15 && cg == 0 && wn == 0 && lane == 0) __builtin_amdgcn_raw_buffer_store_b32(tag, sWS, WS_EPOCH, 0, 16);
    } else {
      lds_wait(smem + L_CNT + 56 + wn * 4, 1u);
      const f2 c1 = *(LAS f2*)(car + 128);
      carry = {c1.x, opq(c1.y)};
    }
    STAMP(7);
    {
      const cf h0 = cfma(S0, carry, E0), h1 = cfma(S1, carry, E1);
      hr = f2{h0.r, h1.r}; hi = f2{-h0.i, -h1.i};
      LAS char* tr = smem + L_TR + q * 1024;
      const unsigned so = WS_HS + (unsigned)(t64 * 32 + cg * 2) * 4096u + (unsigned)wn * 512u;
      const unsigned vo = (unsigned)((g >> 1) * 4096 + (lane & 31) * 16);
      const unsigned qa = L_TR + q * 1024 + (8 * g + q4) * 32 + p4 * 8;
#pragma unroll
      for (int m = 0; m < 4; ++m) {
        unsigned pr[2], pi[2];
#pragma unroll
        for (int j = 0; j < 2; ++j) {
          const int k = 2 * m + j;
          const f2 nr = fma2(lr2, hr, fma2(li2, hi, vr[k])), ni = fma2(lr2, hi, fma2(-li2, hr, vi[k]));
          hr = nr; hi = ni;
          pr[j] = pk_bf(hr.x, hr.y); pi[j] = pk_bf(hi.x, hi.y);
        }
        *(LAS u2*)(tr + r16 * 32 + g * 8) = u2{pr[0], pr[1]};
        *(LAS u2*)(tr + 512 + r16 * 32 + g * 8) = u2{pi[0], pi[1]};
        asm volatile("" ::: "memory");
        const bf8 o = cat(rdtr(qa), rdtr(qa + 128));
        asm volatile("" ::: "memory");
        st128(__builtin_bit_cast(u4, o), sWS, vo + m * 1024, so);
      }
    }
    STAMP(8);
    asm volatile("s_waitcnt vmcnt(0)" ::: "memory");
    __builtin_amdgcn_raw_buffer_store_b32(tag, sWS, lane == 0 ? WS_FLS + (unsigned)(t64 * 32 + cg * 2 + wn) * 128u : OOB, 0, 16);
    __builtin_amdgcn_s_setprio(0);
    STAMP(9);
    if (lane == 0) __hip_atomic_fetch_add((LAS unsigned*)(smem + L_CNT + 12), 1u, __ATOMIC_RELAXED, __HIP_MEMORY_SCOPE_WORKGROUP);
    if (CP < 0) {
#pragma unroll
      for (int j = NC_MID; j < 8; ++j) ldC(j);
    }
    FINAL_LOADS()
    FINAL_PREP()
    pollflags(WS_FLS + (unsigned)(t2 * 32 + q * 8) * 128u, 8, 128u);
    STAMP(10);
    u4 af[8][4];
    const unsigned pw = q * 4096 + r16 * 256 + ((g ^ r16) * 16);
    auto lda = [&](int j, int m) {
      af[j][m] = __builtin_amdgcn_raw_buffer_load_b128(sWS, lane * 16 + m * 1024, WS_HS + (unsigned)(t2 * 32 + q * 8 + j) * 4096u, 16);
    };
#pragma unroll
    for (int j = 0; j < WINS; ++j)
#pragma unroll
      for (int m = 0; m < 4; ++m) lda(j, m);
    SB0();
#pragma unroll
    for (int j = 0; j < 8; ++j) {
      if (j == 7) lds_wait(smem + L_CNT + 16 + q * 4, 1u);
#pragma unroll
      for (int i = 0; i < 16; ++i) {
        if (j == 7 && i >= 5) { const int k = i - 5; *(LAS f4*)(smem + slotoff(2 + (k & 3)) + (pw ^ ((k >> 2) * 64))) = acc2[k >> 2][k & 3]; }
        acc2[i >> 2][i & 3] = mfma(asb(cfr[j][i >> 2]), asb(af[j][i & 3]), acc2[i >> 2][i & 3]);
        if (j >= 1) { const int n = WINS * 4 + (j - 1) * 8 + (i >> 1); if ((i & 1) && n < 32) lda(n >> 2, n & 3); }
        SB0();
      }
    }
    STAMP(11);
#pragma unroll
    for (int k = 11; k < 16; ++k) *(LAS f4*)(smem + slotoff(2 + (k & 3)) + (pw ^ ((k >> 2) * 64))) = acc2[k >> 2][k & 3];
    waitv<63>();
    STAMP(12);
    BAR();
    FINAL_OUT()
    STAMP(13);
    STAMP_OUT();
    asm volatile(".Ls5_end_b:");
    __builtin_amdgcn_endpgm();
  }
}
}

extern "C" void kernel_launch(void* const* d_in, const int* in_sizes, int n_in, void* d_out, int out_size, void* d_ws,
                              size_t ws_size, hipStream_t stream) {
  Params p;
  p.x = d_in[0]; p.lre = d_in[1]; p.lim = d_in[2]; p.bre = d_in[3]; p.bim = d_in[4]; p.cre = d_in[5]; p.cim = d_in[6];
  p.ldt = d_in[7]; p.dsk = d_in[8]; p.wg = d_in[10]; p.wu = d_in[11]; p.wd = d_in[12];
  p.out = (float*)d_out; p.ws = (char*)d_ws;
  s5_glu_kernel<<<256, 512, 0, stream>>>(p);
}
```

```cpp
#include <hip/hip_runtime.h>
#include <stdint.h>

#define SLP_SWEEP 4
#define SLP_FLAG 4
#define PRE_F 4
#define PRE_S 4
#define PFW 0
#define NC_IN 0
#define NF_IN 6
#define LPRIO 3
#define NC_MID 0
#define FLH 5
#define ROT1 0
#define KMUL 1
#define TOUCH 1
#define B01 0
#define ROT2 0
#define AUXA 16
#define NOA 0
#define NOCL 0
#define JOBPOS 2
#define EARLYB 1
#define J0 0
#define TLBW 0
#define HSLP 4
#define WIN0 3

namespace {
typedef __attribute__((ext_vector_type(2))) __bf16 bf2;
typedef __attribute__((ext_vector_type(4))) __bf16 bf4;
typedef __attribute__((ext_vector_type(8))) __bf16 bf8;
typedef __attribute__((ext_vector_type(2))) float f2;
typedef __attribute__((ext_vector_type(4))) float f4;
typedef __attribute__((ext_vector_type(2))) unsigned u2;
typedef __attribute__((ext_vector_type(4))) unsigned u4;
typedef __amdgpu_buffer_rsrc_t srd_t;
#define LAS __attribute__((address_space(3)))
#define DEVI __device__ __forceinline__
template <int N> struct IC { static constexpr int v = N; };

constexpr int SEQ = 2048, DM = 512, FF = 1376;
constexpr unsigned WS_EPOCH = 0;
constexpr unsigned WS_STATS = 4096;
constexpr unsigned WS_FLC = 24576;
constexpr unsigned WS_FLF = 524288;
constexpr unsigned WS_FLS = 655360;
constexpr unsigned WS_CSB = 57344;
constexpr unsigned WS_AGG = 65536;
constexpr unsigned WS_CSW = 327680;
constexpr unsigned WS_CT = 1u << 20;
constexpr unsigned WS_HS = 2u << 20;
constexpr unsigned WS_HF = 6u << 20;
constexpr unsigned WS_END = WS_HF + 32u * 43u * 4096u;
constexpr unsigned OOB = 0x40000000u;
#define STAMP_OFF (64u << 20)
#define NSTAMP 16
constexpr unsigned STG = 49152, L_A = 0, L_SB = 16384, L_WG = 24576, L_WU = 36864;
constexpr unsigned L_RS = 147456;
constexpr unsigned L_NM = 147968;
constexpr unsigned L_PR = 148480;
constexpr unsigned L_CS = 149504;
constexpr unsigned L_CSB = 150272;
constexpr unsigned L_CNT = 150528;
constexpr unsigned L_JUNK = 150784;
constexpr unsigned L_CAR = 151040;
constexpr unsigned L_CP = 151552;
constexpr unsigned L_TR = 155648;
constexpr int LDS_BYTES = 163840;

struct Params {
  const void *x, *lre, *lim, *bre, *bim, *cre, *cim, *ldt, *dsk, *wg, *wu, *wd;
  float* out;
  char* ws;
};

DEVI srd_t mk_srd(const void* p, unsigned bytes) { return __builtin_amdgcn_make_buffer_rsrc((void*)p, 0, bytes, 0x00020000); }
DEVI void glds16(unsigned m0v, unsigned voff, srd_t srd, unsigned soff) {
  asm volatile("s_mov_b32 m0, %0\n\ts_nop 0\n\tbuffer_load_dwordx4 %1, %2, %3 offen lds" ::"s"(m0v), "v"(voff), "s"(srd), "s"(soff) : "memory");
}
DEVI void glds16_sc1(unsigned m0v, unsigned voff, srd_t srd, unsigned soff) {
  asm volatile("s_mov_b32 m0, %0\n\ts_nop 0\n\tbuffer_load_dwordx4 %1, %2, %3 offen sc1 lds" ::"s"(m0v), "v"(voff), "s"(srd), "s"(soff) : "memory");
}
DEVI void glds4(unsigned m0v, unsigned voff, srd_t srd, unsigned soff) {
  asm volatile("s_mov_b32 m0, %0\n\ts_nop 0\n\tbuffer_load_dword %1, %2, %3 offen lds" ::"s"(m0v), "v"(voff), "s"(srd), "s"(soff) : "memory");
}
DEVI void glds4_sc1(unsigned m0v, unsigned voff, srd_t srd, unsigned soff) {
  asm volatile("s_mov_b32 m0, %0\n\ts_nop 0\n\tbuffer_load_dword %1, %2, %3 offen sc1 lds" ::"s"(m0v), "v"(voff), "s"(srd), "s"(soff) : "memory");
}
template <int N> DEVI void waitv() { asm volatile("s_waitcnt vmcnt(%0) lgkmcnt(0)" ::"i"(N) : "memory"); }
#define BAR() do { asm volatile("" ::: "memory"); __builtin_amdgcn_s_barrier(); asm volatile("" ::: "memory"); } while (0)
#define SB0() __builtin_amdgcn_sched_barrier(0)
DEVI float bf_lo(unsigned u) { return __uint_as_float(u << 16); }
DEVI float bf_hi(unsigned u) { return __uint_as_float(u & 0xffff0000u); }
DEVI float rbf(float v) { return (float)(__bf16)v; }
DEVI unsigned pk_bf(float a, float b) { bf2 r = {(__bf16)a, (__bf16)b}; return __builtin_bit_cast(unsigned, r); }
DEVI f4 mfma(bf8 a, bf8 b, f4 c) { return __builtin_amdgcn_mfma_f32_16x16x32_bf16(a, b, c, 0, 0, 0); }
DEVI void st128(u4 v, srd_t srd, unsigned voff, unsigned soff) {
  __builtin_amdgcn_raw_buffer_store_b128(v, srd, voff, soff, 16);
  asm volatile("s_nop 1" ::"v"(v) : "memory");
}
DEVI bf8 asb(u4 v) { return __builtin_bit_cast(bf8, v); }
DEVI f2 lo2(f4 v) { return __builtin_shufflevector(v, v, 0, 1); }
DEVI f2 hi2(f4 v) { return __builtin_shufflevector(v, v, 2, 3); }
DEVI f2 fma2(f2 a, f2 b, f2 c) { return __builtin_elementwise_fma(a, b, c); }
DEVI f2 sp(float a) { return f2{a, a}; }
DEVI u2 swap16(unsigned a) { auto r = __builtin_amdgcn_permlane16_swap(a, a, false, false); return u2{r[0], r[1]}; }
DEVI u2 swap32(unsigned a) { auto r = __builtin_amdgcn_permlane32_swap(a, a, false, false); return u2{r[0], r[1]}; }
DEVI float hsum(float v) {
  const unsigned u = __float_as_uint(v);
  auto b = swap32(u);
  return __uint_as_float(b[0]) + __uint_as_float(b[1]);
}
DEVI float gsum(float v) {
  const unsigned u = __float_as_uint(v);
  auto a = swap16(u);
  return hsum(__uint_as_float(a[0]) + __uint_as_float(a[1]));
}
template <int CTRL> DEVI float dpp_add(float v) {
  return v + __uint_as_float(__builtin_amdgcn_update_dpp(0u, __float_as_uint(v), CTRL, 0xf, 0xf, true));
}
DEVI float wave_sum(float v) {
  v = dpp_add<0xB1>(v);
  v = dpp_add<0x4E>(v);
  v = dpp_add<0x141>(v);
  v = dpp_add<0x140>(v);
  return gsum(v);
}
DEVI void gather4(float v, float& g0, float& g1, float& g2, float& g3) {
  unsigned u = __float_as_uint(v);
  auto a = swap16(u);
  auto e = swap32(a[0]);
  auto o = swap32(a[1]);
  g0 = __uint_as_float(e[0]); g2 = __uint_as_float(e[1]); g1 = __uint_as_float(o[0]); g3 = __uint_as_float(o[1]);
}
struct cf { float r, i; };
DEVI float opq(float v) { asm("" : "+v"(v)); return v; }
DEVI cf cmul(cf a, cf b) { return {fmaf(a.r, b.r, opq(-a.i * b.i)), fmaf(a.r, b.i, a.i * b.r)}; }
DEVI cf csq(cf a) { return {fmaf(a.r, a.r, opq(-a.i * a.i)), opq(a.r + a.r) * a.i}; }
DEVI cf cfma(cf a, cf b, cf c) { return {fmaf(a.r, b.r, opq(fmaf(-a.i, b.i, c.r))), fmaf(a.r, b.i, fmaf(a.i, b.r, c.i))}; }
DEVI int tokmap(int m, int r) { return 16 * (r >> 2) + 8 * (r & 1) + 2 * m + ((r >> 1) & 1); }
DEVI void sincos_cw(float x, float& s, float& c) {
  const float k = rintf(x * 0.63661977236758134f);
  float r = fmaf(k, -1.57079625129699707031f, x);
  r = fmaf(k, -7.54978941586159635335e-08f, r);
  r = fmaf(k, -5.39030285815811905290e-15f, r);
  const float z = r * r;
  float ps = fmaf(z, -1.9515295891e-4f, 8.3321608736e-3f);
  ps = fmaf(z, ps, -1.6666654611e-1f);
  const float sn = fmaf(r * z, ps, r);
  float pc = fmaf(z, 2.443315711809948e-5f, -1.388731625493765e-3f);
  pc = fmaf(z, pc, 4.166664568298827e-2f);
  const float cn = fmaf(z * z, pc, fmaf(z, -0.5f, 1.0f));
  const int q = (int)k;
  const float a = (q & 1) ? cn : sn, b = (q & 1) ? sn : cn;
  s = (q & 2) ? -a : a;
  c = ((q + 1) & 2) ? -b : b;
}

#define STAMP(k) do {} while (0)
#define STAMPC(k) do {} while (0)
#define STAMP_OUT() do {} while (0)

#define FSTAMP(k) do {} while (0)
constexpr unsigned SPIN_CAP = 1u << 18;

#define WINF 5
#define WINS 3
DEVI unsigned lds_ld(LAS char* a) { return __hip_atomic_load((LAS unsigned*)a, __ATOMIC_RELAXED, __HIP_MEMORY_SCOPE_WORKGROUP); }
DEVI void lds_st(LAS char* a, unsigned v) { __hip_atomic_store((LAS unsigned*)a, v, __ATOMIC_RELAXED, __HIP_MEMORY_SCOPE_WORKGROUP); }
DEVI cf sel4(int g, cf a, cf b, cf c, cf d) { return {g == 0 ? a.r : g == 1 ? b.r : g == 2 ? c.r : d.r, g == 0 ? a.i : g == 1 ? b.i : g == 2 ? c.i : d.i}; }
struct Ssm { cf lam, coef2, l8, l16, l64; };
DEVI Ssm ssm_consts(unsigned plre, unsigned plim, unsigned pldt) {
  Ssm c;
  const float dt = rbf(__expf(bf_lo(pldt))), a = bf_lo(plre), b = bf_lo(plim);
  const float e = __expf(dt * a);
  float sn, cn; sincos_cw(dt * b, sn, cn);
  c.lam = {e * cn, e * sn};
  const float nr = c.lam.r - 1.f, ni = c.lam.i, den = 2.0f * __builtin_amdgcn_rcpf(a * a + b * b);
  c.coef2 = {fmaf(nr, a, opq(ni * b)) * den, fmaf(ni, a, -nr * b) * den};
  c.l8 = csq(csq(csq(c.lam))); c.l16 = csq(c.l8); c.l64 = csq(csq(c.l16));
  return c;
}

#if defined(__HIP_DEVICE_COMPILE__)
#define KARG __attribute__((address_space(4)))
#else
#define KARG
#endif
DEVI constexpr unsigned slotoff(int sl) { return (unsigned)(((sl / 3 + 2) % 3) * STG + (sl % 3) * 4096); }
#define LANEVARS() \
  int tid = threadIdx.x; asm volatile("" : "+v"(tid)); \
  const int lane = tid & 63, r16 = lane & 15, g = lane >> 4; \
  const unsigned q4 = r16 >> 2, p4 = lane & 3; \
  const unsigned vD = (unsigned)((lane >> 3) * 1024 + n64 * 128 + ((((lane & 7) >> 1) ^ g) * 32) + (lane & 1) * 16); \
  auto fillp = [&](int j, int sl, int i) { glds16(slotoff(sl) + q * 12288 + i * 1024, vD, sD, (unsigned)((11 * q + kf(j)) * 32 + 8 * i) * 1024u); }; \
  auto fill = [&](int j) { fillp(j, j, 0); fillp(j, j, 1); fillp(j, j, 2); fillp(j, j, 3); }; \
  auto pollflags = [&](unsigned base, int n, unsigned stride) { \
    unsigned a_ = lane < n ? base + lane * stride : OOB; \
    _Pragma("unroll 1") for (unsigned spins = 0; spins < SPIN_CAP; ++spins) { \
      const unsigned f = __builtin_amdgcn_raw_buffer_load_b32(sWS, a_, 0, 16); \
      if (f == tag) a_ = OOB; \
      if (__all(a_ == OOB)) break; \
      __builtin_amdgcn_s_sleep(SLP_FLAG); \
    } \
    asm volatile("" ::: "memory"); \
  };
#define PREAMBLE() \
  const KARG Params* kp = (const KARG Params*)(uintptr_t)__builtin_amdgcn_kernarg_segment_ptr(); asm volatile("" : "+s"(kp)); \
  const Params p = *kp; \
  int bid = blockIdx.x, w = w_; asm volatile("" : "+s"(bid), "+s"(w)); \
  const int q = w & 3, xcd = bid & 7, slot = bid >> 3; \
  const srd_t sWS = mk_srd(p.ws, WS_END); \
  const int tb = (xcd >> 2) * 8 + (slot >> 2), cg = (xcd & 3) * 4 + (slot & 3); \
  const int t2 = xcd * 4 + (slot >> 3), n64 = slot & 7; \
  const int wm = q & 1, wn = q >> 1, t64 = tb * 2 + wm; \
  const bool has3 = (cg & 3) < 3 && cg != 14; \
  const int fb0 = 11 * (cg >> 2) + 3 * (cg & 3) - (cg == 15); \
  const srd_t sX = mk_srd(p.x, SEQ * DM * 2); \
  const srd_t sD = mk_srd(p.wd, FF * DM * 2); \
  const int rot2 = ROT2 == 1 ? (n64 + 2 * t2) & 7 : ROT2 == 2 ? (n64 & 1) * 4 : ROT2 == 3 ? 2 * (t2 & 3) : 0, rotf = rot2 + (rot2 >= 3) + (rot2 >= 6); \
  auto kf = [&](int j) -> int { j += rotf; return j >= 11 ? j - 11 : j; }; \
  unsigned tag; \
  f4 acc2[4][4] = {};
DEVI bf8 cat(bf4 a, bf4 b) { return __builtin_shufflevector(a, b, 0, 1, 2, 3, 4, 5, 6, 7); }
DEVI void lds_wait(LAS char* a, unsigned n) { while (lds_ld(a) < n) __builtin_amdgcn_s_sleep(1); asm volatile("" ::: "memory"); }
DEVI void lds_add64(unsigned addr) {
  unsigned long long keep;
  asm volatile("s_waitcnt lgkmcnt(0)\n\ts_mov_b64 %0, exec\n\ts_mov_b64 exec, 1\n\tds_add_u32 %1, %2\n\ts_mov_b64 exec, %0" : "=&s"(keep) : "v"(addr), "v"(64u) : "memory");
}
template <unsigned N> DEVI void lds_spin(unsigned addr) {
  unsigned v, sv;
  asm volatile("1:\n\tds_read_b32 %0, %2\n\ts_waitcnt lgkmcnt(0)\n\tv_readfirstlane_b32 %1, %0\n\ts_nop 1\n\ts_cmp_ge_u32 %1, %3\n\ts_cbranch_scc0 1b"
               : "=&v"(v), "=&s"(sv) : "v"(addr), "i"(N) : "memory", "scc");
}
DEVI void lds_post(LAS char* a) { asm volatile("s_waitcnt lgkmcnt(0)" ::: "memory"); lds_st(a, 1u); }
DEVI f4 unpk(u2 v) { return f4{bf_lo(v.x), bf_hi(v.x), bf_lo(v.y), bf_hi(v.y)}; }

__global__ __launch_bounds__(512, 2) void s5_glu_kernel(Params p_) {
  __shared__ __attribute__((aligned(16))) char smem_[LDS_BYTES];
  LAS char* smem = (LAS char*)smem_;
  auto rd128 = [&](unsigned addr) -> bf8 { return *(LAS bf8*)(smem + addr); };
  auto rdtr = [&](unsigned addr) -> bf4 { return __builtin_amdgcn_ds_read_tr16_b64_v4bf16((LAS bf4*)(smem + addr)); };
  asm volatile(".Ls5_start:");
  STAMP(0);
  const int w_ = __builtin_amdgcn_readfirstlane(threadIdx.x >> 6);
  {
    unsigned long long pc; unsigned len, back;
    asm volatile("s_getpc_b64 %0\n.Ls5_pf_here:\n\ts_mov_b32 %1, .Ls5_end_a-.Ls5_start\n\ts_max_i32 %1, %1, .Ls5_end_b-.Ls5_start\n\ts_mov_b32 %2, .Ls5_pf_here-.Ls5_start" : "=s"(pc), "=s"(len), "=s"(back)::"scc");
    const srd_t sI = mk_srd((const void*)(pc - back), w_ == 0 ? len : 0u);
    glds4(L_JUNK, (unsigned)(((threadIdx.x & 63) * 32 + (blockIdx.x >> 3)) * 128), sI, 0);
  }
  if (w_ >= 4) {
    PREAMBLE()
    LANEVARS()
    const unsigned ch = (unsigned)(cg * 32 + wn * 16 + r16);
    auto ldparams = [&](unsigned& pa, unsigned& pb, unsigned& pc) {
      const srd_t s1 = mk_srd(p.lre, 1024), s2 = mk_srd(p.lim, 1024), s3 = mk_srd(p.ldt, 1024);
      asm volatile("s_nop 4\n\tbuffer_load_ushort %0, %3, %4, 0 offen\n\tbuffer_load_ushort %1, %3, %5, 0 offen\n\tbuffer_load_ushort %2, %3, %6, 0 offen"
                   : "=&v"(pa), "=&v"(pb), "=&v"(pc) : "v"(ch * 2u), "s"(s1), "s"(s2), "s"(s3) : "memory");
    };
#define NEXT(n) do { waitv<n>(); BAR(); } while (0)
    Ssm K; cf l512, pwg, S0, S1, wT;
    auto consts_a = [&](unsigned pa, unsigned pb, unsigned pc) {
      K = ssm_consts(pa, pb, pc);
      asm volatile("" : "+v"(K.lam.r), "+v"(K.lam.i), "+v"(K.coef2.r), "+v"(K.coef2.i), "+v"(K.l8.r), "+v"(K.l8.i), "+v"(K.l16.r), "+v"(K.l16.i), "+v"(K.l64.r), "+v"(K.l64.i));
    };
    auto consts_b = [&]() {
      const cf l32 = csq(K.l16), l48 = cmul(l32, K.l16), l128 = csq(K.l64), l256 = csq(l128), l384 = cmul(l256, l128);
      l512 = csq(l256); pwg = sel4(g, cf{1.f, 0.f}, l128, l256, l384); wT = sel4(g, l48, l32, K.l16, cf{1.f, 0.f});
      S0 = cmul(K.coef2, sel4(g, cf{1.f, 0.f}, K.l16, l32, l48)); S1 = cmul(K.l8, S0);
      asm volatile("" : "+v"(l512.r), "+v"(l512.i), "+v"(pwg.r), "+v"(pwg.i), "+v"(S0.r), "+v"(S0.i), "+v"(S1.r), "+v"(S1.i), "+v"(wT.r), "+v"(wT.i));
    };
    if (q < 3) {
      unsigned pa, pb, pc;
      ldparams(pa, pb, pc);
      const srd_t sa = mk_srd(q == 0 ? p.x : q == 1 ? p.wg : p.wu, q == 0 ? SEQ * DM * 2 : DM * FF * 2);
      const srd_t sb = mk_srd(q == 0 ? p.x : p.bre, q == 0 ? SEQ * DM * 2 : DM * DM * 2), sc = mk_srd(q == 0 ? p.x : p.bim, q == 0 ? SEQ * DM * 2 : DM * DM * 2);
      const unsigned stepa = q == 0 ? 128u : 64u * FF * 2, ma = q == 0 ? L_A : q == 1 ? L_WG : L_WU, mb = q == 0 ? L_A + 12288u : L_SB + (q - 1) * 4096u;
      unsigned vo[16];
#pragma unroll
      for (int i = 0; i < 16; ++i) {
        const int r = 8 * (i & 1) + (lane >> 3);
        const unsigned sw = (unsigned)(((lane & 7) ^ ((r >> 1) & 7)) * 16);
        const unsigned va = (unsigned)(tb * 128 + (i >> 3) * 64 + tokmap((i >> 1) & 3, r)) * 1024u + sw;
        unsigned vb;
        if (i < 12) {
          const int idx = 64 * i + lane, k = idx / 12, c = idx - 12 * k, u = (c >> 1) ^ ((k >> 3) & 1);
          vb = (!has3 && u >= 4) ? OOB : (unsigned)(k * (FF * 2) + (fb0 * 32 + 16 * u + 8 * (c & 1)) * 2);
        } else vb = (unsigned)(cg * 32 + (q - 1) * 16 + r) * 1024u + sw;
        vo[i] = q == 0 ? va : vb;
        if (i < 12) glds16(ma + i * 1024, vo[i], sa, 0); else glds16(mb + (i - 12) * 1024, vo[i], i < 14 ? sb : sc, 0);
      }
      auto eight = [&](int hf, int s) {
#pragma unroll
        for (int j = 0; j < 8; ++j) {
          const int i = j < 6 ? hf * 6 + j : 12 + hf * 2 + (j - 6);
          if (i < 12) glds16((s % 3) * STG + ma + i * 1024, vo[i], sa, stepa * s); else glds16((s % 3) * STG + mb + (i - 12) * 1024, vo[i], i < 14 ? sb : sc, 128u * s);
        }
      };
      auto fills = [&](int sl0) {
#pragma unroll
        for (int i = 0; i < 16; ++i) glds16(slotoff(sl0) + q * 4096 + (i >> 2) * 12288 + (i & 3) * 1024, vD, sD, (unsigned)((11 * (i >> 2) + sl0 + q) * 32 + 8 * (i & 3)) * 1024u);
      };
      STAMP(1);
      eight(0, 1);
      asm volatile("s_waitcnt vmcnt(8)" : "+v"(pa), "+v"(pb), "+v"(pc)::"memory");
      STAMP(2);
      BAR();
      STAMP(3);
      eight(1, 1); eight(0, 2); NEXT(8);
      FSTAMP(7);
      eight(1, 2); eight(0, 3); NEXT(8);
      FSTAMP(8);
      eight(1, 3); eight(0, 4); eight(1, 4); NEXT(16);
      FSTAMP(9);
      eight(0, 5); eight(1, 5); consts_a(pa, pb, pc); NEXT(16);
      STAMP(4);
      eight(0, 6); eight(1, 6); consts_b(); NEXT(16);
      FSTAMP(10);
      eight(0, 7); eight(1, 7); NEXT(16);
      FSTAMP(11);
      asm volatile("s_waitcnt vmcnt(0)" ::: "memory");
      lds_add64(L_CNT + 8);
      fills(0);
      FSTAMP(12);
      lds_wait(smem + L_CNT + 12, 256u);
      fills(3);
      tag = __builtin_amdgcn_readfirstlane(lds_ld(smem + L_CNT + 4));
    } else {
      unsigned pa, pb, pc;
      ldparams(pa, pb, pc);
      const int jx = xcd & 3, jn = jx == 3 ? 10 : 11, ji = ((slot & 3) ^ 3) * 8 + (slot >> 2), jmat = ji >= jn, jl = ji - jn * jmat, jkh = xcd >> 2;
      const int je = 6 - (jx & 1), jln = jl < je ? (11 * jx + 1) / 2 + jl : 22 + (11 * jx) / 2 + (jl - je);
      const bool jon = ji < 2 * jn;
      const int cn64 = bid & 7, cks = bid >> 3;
      if (TLBW) {
        const unsigned o = (unsigned)(lane * 32 + slot) * 65536u;
        glds4(L_JUNK, lane < 6 ? o : OOB, sWS, 0);
        glds4(L_JUNK, lane < 2 ? o : OOB, mk_srd(p.out, SEQ * DM * 4), 0);
        glds4(L_JUNK, lane < 1 ? o : OOB, sD, 0);
        glds4(L_JUNK, lane < 1 ? (o & 0x70000u) : OOB, mk_srd(slot < 8 ? p.cre : slot < 16 ? p.cim : p.dsk, slot < 16 ? DM * DM * 2 : DM * 2), 0);
      }
      asm volatile("s_waitcnt vmcnt(%3)" : "+v"(pa), "+v"(pb), "+v"(pc) : "i"(TLBW ? 4 : 0) : "memory");
      consts_a(pa, pb, pc); consts_b();
      u4 wj[16];
      {
        const srd_t sJ = mk_srd(jmat ? p.wu : p.wg, jon ? DM * FF * 2 : 0);
        const unsigned vj = (unsigned)((lane >> 3) * 5504 + (lane & 7) * 16), sj = (unsigned)(jln * 128), s0 = (jkh ? 128u : 64u) * 5504u, s1 = (jkh ? 192u : 0u) * 5504u;
        if (J0 == 1) BAR();
#pragma unroll
        for (int i = 0; i < 8; ++i) wj[i] = __builtin_amdgcn_raw_buffer_load_b128(sJ, vj, sj + s0 + i * 8 * 5504, 0);
        STAMP(1); STAMP(2);
        BAR();
        STAMP(3);
#pragma unroll
        for (int i = 0; i < 8; ++i) wj[8 + i] = __builtin_amdgcn_raw_buffer_load_b128(sJ, vj, sj + s1 + i * 8 * 5504, 0);
        if (J0 == 0) BAR();
      }
      tag = __builtin_amdgcn_readfirstlane(lds_ld(smem + L_CNT + 4));
      BAR();
      {
        u4 sel;
#pragma unroll
        for (int i = 0; i < 4; ++i) sel[i] = (r16 >> 1) == i ? ((r16 & 1) ? 0x3f800000u : 0x00003f80u) : 0u;
        f4 d = {0.f, 0.f, 0.f, 0.f};
        asm volatile("" : "+v"(sel) :: "memory");
#pragma unroll
        for (int i = 0; i < 8; ++i) d = mfma(asb(wj[i]), asb(sel), d);
        asm volatile("" : "+v"(d));
        BAR();
#pragma unroll
        for (int i = 8; i < 16; ++i) d = mfma(asb(wj[i]), asb(sel), d);
        const unsigned so = jon ? WS_CSW + (unsigned)((jmat * 2 + jkh) * 2752 + jln * 64) * 8u : OOB;
#pragma unroll
        for (int j = 0; j < 4; ++j) __builtin_amdgcn_raw_buffer_store_b64(u2{__float_as_uint(hsum(d[j])), tag}, sWS, (lane & 0x28) == 0 ? (unsigned)(8 * (4 * g + j) + r16) * 8u : OOB, so, 16);
      }
      BAR();
      STAMP(4);
      const unsigned gb = WS_STATS + (unsigned)(tb * 128 + 2 * lane) * 8u, gc = WS_CSB + (unsigned)(((lane >> 4) & 1) * 512 + cg * 32 + 2 * r16) * 8u;
      u4 g0 = __builtin_amdgcn_raw_buffer_load_b128(sWS, gb, 0, 16), g1 = __builtin_amdgcn_raw_buffer_load_b128(sWS, gc, 0, 16);
      BAR();
#pragma unroll 1
      for (unsigned spins = 0; spins < SPIN_CAP && !__all((g0.y == tag) & (g0.w == tag) & (g1.y == tag) & (g1.w == tag)); ++spins) {
        g0 = __builtin_amdgcn_raw_buffer_load_b128(sWS, gb, 0, 16); g1 = __builtin_amdgcn_raw_buffer_load_b128(sWS, gc, 0, 16);
        asm volatile("" : "+v"(g0), "+v"(g1));
      }
#pragma unroll
      for (int s = 0; s < 2; ++s) {
        const unsigned v = s ? g0.z : g0.x;
        const int rho = (lane & 32) * 2 + (lane & 3) * 16 + ((lane >> 3) & 3) * 4 + 2 * s + ((lane >> 2) & 1);
        const float rs = __builtin_amdgcn_rcpf(bf_hi(v)), nm = -bf_lo(v);
        *(LAS float*)(smem + L_RS + rho * 4) = rs;
        *(LAS float*)(smem + L_NM + rho * 4) = rs * nm;
        *(LAS f2*)(smem + L_PR + rho * 8) = f2{rs * rs, nm};
      }
      *(LAS u2*)(smem + L_CSB + (lane & 31) * 8) = u2{g1.x, g1.z};
      __builtin_amdgcn_s_sleep(HSLP);
      glds4(L_JUNK, lane < 43 ? (unsigned)(bid * 43 + lane) * 128u : OOB, sD, 0);
#pragma unroll
      for (int i = 0; i < 4; ++i) {
        const int r = 8 * (i & 1) + (lane >> 3);
        glds16(L_TR + i * 1024, (unsigned)(r * 1024 + ((lane & 7) ^ ((r >> 1) & 7)) * 16), mk_srd((cks & 2) ? p.cim : p.cre, DM * DM * 2), (unsigned)((cn64 * 64 + (cks & 1) * 32 + (i >> 1) * 16) * 1024 + (cks >> 2) * 128));
      }
      glds4_sc1(L_JUNK, ((lane & 32) ? WS_FLS : WS_FLF) + (unsigned)(t2 * 32 + (lane & 31)) * 128u, sWS, 0);
      glds4_sc1(L_JUNK, (lane >> 2) < tb ? WS_AGG + (unsigned)((lane >> 2) * 8192 + cg * 512 + (lane & 3) * 128) : OOB, sWS, 0);
      NEXT(63);
      u4 gw[8];
      const unsigned ga = lane < (has3 ? 48 : 32) ? WS_CSW + (unsigned)(fb0 * 32 + 2 * lane) * 8u : OOB;
#pragma unroll
      for (int i = 0; i < 8; ++i) gw[i] = __builtin_amdgcn_raw_buffer_load_b128(sWS, ga, i * 1376 * 8, 16);
#pragma unroll 1
      for (unsigned spins = 0; spins < SPIN_CAP; ++spins) {
        bool ok = true;
#pragma unroll
        for (int i = 0; i < 8; ++i) ok &= (gw[i].y == tag) & (gw[i].w == tag);
        if (__builtin_expect(__all(ok | (ga == OOB)), 1)) break;
        __builtin_amdgcn_s_sleep(SLP_FLAG);
#pragma unroll
        for (int i = 0; i < 8; ++i) gw[i] = __builtin_amdgcn_raw_buffer_load_b128(sWS, ga, i * 1376 * 8, 16);
      }
      if (lane < 48) {
#pragma unroll
        for (int m = 0; m < 2; ++m)
          *(LAS f2*)(smem + L_CS + m * 384 + lane * 8) = f2{(__uint_as_float(gw[4 * m].x) + __uint_as_float(gw[4 * m + 1].x)) + (__uint_as_float(gw[4 * m + 2].x) + __uint_as_float(gw[4 * m + 3].x)),
                                                            (__uint_as_float(gw[4 * m].z) + __uint_as_float(gw[4 * m + 1].z)) + (__uint_as_float(gw[4 * m + 2].z) + __uint_as_float(gw[4 * m + 3].z))};
      }
      lds_post(smem + L_CNT + 84);
      asm volatile("" ::: "memory");
#pragma unroll
      for (int i = 0; i < 4; ++i)
        st128(__builtin_bit_cast(u4, rd128(L_TR + (i >> 1) * 2048 + ((unsigned)(r16 * 128 + ((g ^ (r16 >> 1)) * 16)) ^ ((i & 1) * 64)))), sWS, lane * 16,
              WS_CT + (unsigned)(((cn64 * 32 + (cks >> 2) * 4 + (i & 1) * 2 + ((cks >> 1) & 1)) * 4 + (cks & 1) * 2 + (i >> 1)) * 1024));
      waitv<63>();
      lds_st(smem + L_CNT + 88, 1u);
    }
    u4 cfr[8][4];
    auto ldC = [&](int j) {
#pragma unroll
      for (int nt = 0; nt < 4; ++nt) cfr[j][nt] = __builtin_amdgcn_raw_buffer_load_b128(sWS, lane * 16 + nt * 1024, WS_CT + (unsigned)(n64 * 32 + q * 8 + j) * 4096u, 16);
    };
    f4 rs[4], br[4], bi[4];
    {
      const float csr = *(LAS float*)(smem + L_CSB + (wn * 16 + r16) * 4), csi = opq(*(LAS float*)(smem + L_CSB + (32 + wn * 16 + r16) * 4));
#pragma unroll
      for (int m = 0; m < 4; ++m) {
        rs[m] = *(LAS f4*)(smem + L_RS + (wm * 64 + 16 * m + 4 * g) * 4);
        const f4 t = *(LAS f4*)(smem + L_NM + (wm * 64 + 16 * m + 4 * g) * 4);
        br[m] = t * csr; bi[m] = t * csi;
        asm volatile("" : "+v"(br[m]), "+v"(bi[m]), "+v"(rs[m]));
      }
    }
    STAMP(5);
#define LQUIET 0

    __builtin_amdgcn_s_setprio(LPRIO);
    f2 vr[8], vi[8];
    const f2 lr2 = sp(K.lam.r), li2 = sp(K.lam.i);
    lds_wait(smem + L_CNT + 16 + q * 4, 1u);
#pragma unroll
    for (int m = 0; m < 4; ++m) {
      const f4 xr = *(LAS f4*)(smem + STG + q * 12288 + m * 1024 + lane * 16), xi = *(LAS f4*)(smem + STG + q * 12288 + 4096 + m * 1024 + lane * 16);
      vr[2 * m] = fma2(lo2(rs[m]), lo2(xr), lo2(br[m])); vr[2 * m + 1] = fma2(hi2(rs[m]), hi2(xr), hi2(br[m]));
      vi[2 * m] = fma2(lo2(rs[m]), lo2(xi), lo2(bi[m])); vi[2 * m + 1] = fma2(hi2(rs[m]), hi2(xi), hi2(bi[m]));
    }
    f2 hr = vr[0], hi = vi[0];
#pragma unroll
    for (int k = 1; k < 8; ++k) {
      const f2 nr = fma2(lr2, hr, fma2(-li2, hi, vr[k])), ni = fma2(lr2, hi, fma2(li2, hr, vi[k]));
      hr = nr; hi = ni;
    }
    const cf runA = {hr.x, hi.x};
    const cf agg = cfma(K.l8, runA, cf{hr.y, hi.y});
    cf T = cmul(wT, agg);
    T = {gsum(T.r), gsum(T.i)};
    LAS char* car = smem + L_CAR + wn * 256 + r16 * 8;
    if (wm == 0) {
      *(LAS f2*)car = f2{T.r, T.i};
      lds_post(smem + L_CNT + 48 + wn * 4);
      asm volatile("s_waitcnt vmcnt(0)" ::: "memory");
    } else {
      lds_wait(smem + L_CNT + 48 + wn * 4, 1u);
      const f2 t0 = *(LAS f2*)car;
      const cf W = cfma(K.l64, cf{t0.x, opq(t0.y)}, T);
      st128(u4{__float_as_uint(W.r), tag, __float_as_uint(W.i), tag}, sWS, g == 0 ? ch * 16u : OOB, WS_AGG + (unsigned)tb * 8192u);
      asm volatile("s_waitcnt vmcnt(1)" ::: "memory");
    }
    if (lane == 0) __hip_atomic_fetch_add((LAS unsigned*)(smem + L_CNT + 32), 1u, __ATOMIC_RELAXED, __HIP_MEMORY_SCOPE_WORKGROUP);
    __builtin_amdgcn_raw_buffer_store_b32(tag, sWS, (q == 3 && lane == 0) ? WS_FLC + (unsigned)((bid & 7) * 32 + (bid >> 3)) * 4u : OOB, 0, 16);
    asm volatile("" ::: "memory");
    __builtin_amdgcn_s_setprio(0);
    STAMP(6);
    const f2 cr2 = sp(K.coef2.r), ci2 = sp(K.coef2.i);
#pragma unroll
    for (int k = 0; k < 8; ++k) {
      if (k >= NC_IN && k < NC_MID) ldC(k);
      const f2 a = fma2(cr2, vr[k], -(ci2 * vi[k])), b = -fma2(cr2, vi[k], ci2 * vr[k]);
      vr[k] = a; vi[k] = b;
      asm volatile("" : "+v"(vr[k]), "+v"(vi[k])::"memory");
    }
    cf E0, E1;
    {
      cf A0, A1, A2, A3;
      gather4(agg.r, A0.r, A1.r, A2.r, A3.r);
      gather4(agg.i, A0.i, A1.i, A2.i, A3.i);
      const cf P2 = cfma(K.l16, A0, A1), P3 = cfma(K.l16, P2, A2);
      E0 = cmul(K.coef2, sel4(g, cf{0.f, 0.f}, A0, P2, P3)); E1 = cfma(K.l8, E0, cmul(K.coef2, runA));
      asm volatile("" : "+v"(E0.r), "+v"(E0.i), "+v"(E1.r), "+v"(E1.i));
    }
    STAMP(7);
    __builtin_amdgcn_s_setprio(LPRIO);
    cf carry;
    if (wm == 0) {
      u4 Xg[4]; unsigned ad[4];
#pragma unroll
      for (int i = 0; i < 4; ++i) { const int src = tb - 1 - (4 * i + g); ad[i] = src >= 0 ? WS_AGG + (unsigned)src * 8192u + ch * 16u : OOB; }
      unsigned todo = 0xfu;
#pragma unroll 1
      for (unsigned spins = 0; todo && spins < SPIN_CAP; ++spins) {
#pragma unroll
        for (int i = 0; i < 4; ++i) if (todo >> i & 1) Xg[i] = __builtin_amdgcn_raw_buffer_load_b128(sWS, ad[i], 0, 16);
        unsigned left = 0;
#pragma unroll
        for (int i = 0; i < 4; ++i) left |= __all((ad[i] == OOB) | ((Xg[i].y == tag) & (Xg[i].w == tag))) ? 0u : 1u << i;
        todo = left;
        if (todo) __builtin_amdgcn_s_sleep(SLP_SWEEP);
      }
      cf c = {__uint_as_float(Xg[3].x), __uint_as_float(Xg[3].z)};
#pragma unroll
      for (int i = 2; i >= 0; --i) c = cfma(l512, c, cf{__uint_as_float(Xg[i].x), __uint_as_float(Xg[i].z)});
      c = cmul(c, pwg);
      carry = {gsum(c.r), gsum(c.i)};
      const cf c1 = cfma(K.l64, carry, T);
      *(LAS f2*)(car + 128) = f2{c1.r, c1.i};
      lds_post(smem + L_CNT + 56 + wn * 4);
      if (tb == 15 && cg == 0 && wn == 0 && lane == 0) __builtin_amdgcn_raw_buffer_store_b32(tag, sWS, WS_EPOCH, 0, 16);
    } else {
      lds_wait(smem + L_CNT + 56 + wn * 4, 1u);
      const f2 c1 = *(LAS f2*)(car + 128);
      carry = {c1.x, opq(c1.y)};
    }
    STAMP(8);
    {
      lds_wait(smem + L_CNT + 88, 1u);
      carry = {opq(carry.r), opq(carry.i)};
      const cf h0 = cfma(S0, carry, E0), h1 = cfma(S1, carry, E1);
      hr = f2{h0.r, h1.r}; hi = f2{-h0.i, -h1.i};
      const unsigned so = WS_HS + (unsigned)(t64 * 32 + cg * 2) * 4096u + (unsigned)wn * 512u;
      const unsigned vo = (unsigned)((g >> 1) * 4096 + (lane & 31) * 16);
      const unsigned wa = L_TR + q * 2048 + r16 * 32 + g * 8, qa = L_TR + q * 2048 + (8 * g + q4) * 32 + p4 * 8;
      bf8 o;
#pragma unroll
      for (int m = 0; m < 4; ++m) {
        unsigned pr[2], pi[2];
#pragma unroll
        for (int j = 0; j < 2; ++j) {
          const int k = 2 * m + j;
          const f2 nr = fma2(lr2, hr, fma2(li2, hi, vr[k])), ni = fma2(lr2, hi, fma2(-li2, hr, vi[k]));
          hr = nr; hi = ni;
          pr[j] = pk_bf(hr.x, hr.y); pi[j] = pk_bf(hi.x, hi.y);
        }
        if (m > 0) st128(__builtin_bit_cast(u4, o), sWS, vo + (m - 1) * 1024, so);
        *(LAS u2*)(smem + wa + (m & 1) * 1024) = u2{pr[0], pr[1]};
        *(LAS u2*)(smem + wa + (m & 1) * 1024 + 512) = u2{pi[0], pi[1]};
        asm volatile("" ::: "memory");
        o = cat(rdtr(qa + (m & 1) * 1024), rdtr(qa + (m & 1) * 1024 + 128));
        asm volatile("" ::: "memory");
      }
      st128(__builtin_bit_cast(u4, o), sWS, vo + 3 * 1024, so);
    }
    STAMP(9);
    asm volatile("s_waitcnt vmcnt(0)" ::: "memory");
    __builtin_amdgcn_raw_buffer_store_b32(tag, sWS, lane == 0 ? WS_FLS + (unsigned)(t64 * 32 + cg * 2 + wn) * 128u : OOB, 0, 16);
    __builtin_amdgcn_s_setprio(0);
    STAMP(10);
    pollflags(WS_FLC + (unsigned)(n64 * 128 + q * 32), 8, 4u);
#pragma unroll
    for (int j = (NC_MID > NC_IN ? NC_MID : NC_IN); j < 8; ++j) ldC(j);
    __builtin_amdgcn_s_sleep(PRE_S);
    pollflags(WS_FLS + (unsigned)(t2 * 32 + q * 8) * 128u, 8, 128u);
    STAMP(11);
    u4 af[8][4];
    const unsigned pw = q * 12288 + r16 * 256 + ((g ^ r16) * 16);
    auto lda = [&](int j, int m) {
      af[j][m] = __builtin_amdgcn_raw_buffer_load_b128(sWS, NOA > 1 ? OOB : lane * 16 + m * 1024, WS_HS + (unsigned)(t2 * 32 + q * 8 + ((j + rot2) & 7)) * 4096u, AUXA);
    };
#pragma unroll
    for (int n = 0; n < WINS * 4; ++n) lda(n >> 2, n & 3);
    SB0();
#pragma unroll
    for (int j = 0; j < 8; ++j) {
      if (j == 7) lds_wait(smem + L_CNT + 64 + q * 4, 1u);
#pragma unroll
      for (int i = 0; i < 16; ++i) {
        if (j == 7 && i >= 5) { const int k = i - 5; *(LAS f4*)(smem + slotoff(2 + (k & 3)) + (pw ^ ((k >> 2) * 64))) = acc2[k >> 2][k & 3]; }
        acc2[i >> 2][i & 3] = mfma(asb(cfr[j][i >> 2]), asb(af[j][i & 3]), acc2[i >> 2][i & 3]);
        if (j >= 1) { const int n = WINS * 4 + (j - 1) * 8 + (i >> 1); if ((i & 1) && n < 32) lda(n >> 2, n & 3); }
        SB0();
      }
    }
    STAMP(12);
#pragma unroll
    for (int k = 11; k < 16; ++k) *(LAS f4*)(smem + slotoff(2 + (k & 3)) + (pw ^ ((k >> 2) * 64))) = acc2[k >> 2][k & 3];
    waitv<63>();
    BAR();
    STAMP(13);
    if (!LQUIET) STAMP_OUT();
    asm volatile(".Ls5_end_b:");
    __builtin_amdgcn_endpgm();
  } else {
    PREAMBLE()
    f4 ag[4][4] = {}, av[2][4] = {};
    {
      LANEVARS()
      {
        const int brow = (tb >> 3) * 512 + cg * 32 + (tb & 7) * 4 + q, xr = tb * 128 + cg * 8 + 2 * q;
        u4 xa[2];
#pragma unroll
        for (int i = 0; i < 2; ++i) xa[i] = __builtin_amdgcn_raw_buffer_load_b128(sX, lane * 16 + i * 1024, (unsigned)xr * 1024u, 0);
        const unsigned pfw = __builtin_amdgcn_raw_buffer_load_b32(mk_srd(q < 2 ? p.wg : p.wu, DM * FF * 2), lane < 6 ? (unsigned)((bid * 2 + (q & 1)) * 6 + lane) * 128u : OOB, 0, 0);
        const u4 br = __builtin_amdgcn_raw_buffer_load_b128(mk_srd((brow >> 9) ? p.bim : p.bre, DM * DM * 2), lane * 16, (unsigned)(brow & 511) * 1024u, 0);
        const unsigned ep = __builtin_amdgcn_raw_buffer_load_b32(sWS, 0, WS_EPOCH, 16);
        SB0();
        STAMP(1);
        float mean[2], sd[2];
#pragma unroll
        for (int i = 0; i < 2; ++i) {
          float v[8];
#pragma unroll
          for (int j = 0; j < 4; ++j) { v[2 * j] = bf_lo(xa[i][j]); v[2 * j + 1] = bf_hi(xa[i][j]); }
          mean[i] = wave_sum(((v[0] + v[1]) + (v[2] + v[3])) + ((v[4] + v[5]) + (v[6] + v[7]))) * (1.0f / DM);
          float qq = 0.f;
#pragma unroll
          for (int j = 0; j < 8; ++j) { const float dd = v[j] - mean[i]; qq = fmaf(dd, dd, qq); }
          sd[i] = __builtin_amdgcn_sqrtf(rbf(rbf(wave_sum(qq) * (1.0f / DM)) + 1e-5f));
        }
        asm volatile("" ::"v"(pfw));
        tag = __builtin_amdgcn_readfirstlane(ep) + 1u;
        if (tid < 32) lds_st(smem + L_CNT + tid * 4, tid == 1 ? tag : 0u);
        st128(u4{pk_bf(mean[0], sd[0]), tag, pk_bf(mean[1], sd[1]), tag}, sWS, lane == 0 ? WS_STATS + (unsigned)xr * 8u : OOB, 0);
        float s = 0.f;
#pragma unroll
        for (int j = 0; j < 4; ++j) s += bf_lo(br[j]) + bf_hi(br[j]);
        s = wave_sum(s);
        __builtin_amdgcn_raw_buffer_store_b64(u2{__float_as_uint(s), tag}, sWS, lane == 0 ? WS_CSB + (unsigned)brow * 8u : OOB, 0, 16);
      }
      f4 as[2][4] = {};
      unsigned ad[10];
      const unsigned aA0 = (unsigned)(r16 * 128 + ((g ^ (r16 >> 1)) * 16));
      ad[0] = (aA0 + wm * 8192) ^ (wn * 4096); ad[1] = ad[0] ^ 64u; ad[2] = ad[0] ^ 4096u; ad[3] = ad[2] ^ 64u;
      ad[4] = L_SB + aA0 + wn * 4096; ad[5] = ad[4] ^ 64u;
      ad[8] = L_WG + (unsigned)((8 * g + q4) * 192 + 8 * p4 + (g & 1) * 32) + 128u; ad[9] = ad[8] ^ 32u;
      ad[6] = ad[8] - 128u + wn * 64u; ad[7] = ad[6] ^ 32u;
#pragma unroll
      for (int i = 0; i < 10; ++i) asm volatile("" : "+v"(ad[i]));
      bf8 fa[2][4], fs[2], fw[4], fv[2][4];
      auto wfrag = [&](int b, int i, unsigned off) -> bf8 {
        const unsigned a = ad[b + (i & 1)] + (i >> 1) * 12288 + off;
        return cat(rdtr(a), rdtr(a + 768));
      };
      auto step = [&](auto H_) {
        constexpr int H = decltype(H_)::v, c = H & 1, n = c ^ 1;
        constexpr int kk = (H + 1) & 1;
        if ((H & 1) && H < 13) { waitv<63>(); BAR(); }
        if (H == 13) { lds_add64(L_CNT + 12); lds_spin<192>(L_CNT + 8); }
        if (H == 15) { lds_add64(L_CNT + 80); lds_spin<256>(L_CNT + 80); }
        unsigned ho = 0;
        if (H == 15) { int t_ = threadIdx.x; asm volatile("" : "+v"(t_)); ho = STG + q * 12288 + wn * 2048 + (t_ & 63) * 16; }
        SB0();
#pragma unroll
        for (int i = 0; i < 32; ++i) {
          if (i < 8) as[i >> 2][i & 3] = mfma(fa[c][i & 3], fs[i >> 2], as[i >> 2][i & 3]);
          else if (i < 24) ag[i & 3][(i - 8) >> 2] = mfma(fw[(i - 8) >> 2], fa[c][i & 3], ag[i & 3][(i - 8) >> 2]);
          else av[i & 1][(i - 24) >> 1] = mfma(fv[c][(i - 24) >> 1], fa[c][i & 1], av[i & 1][(i - 24) >> 1]);
          if (H < 15) {
            if (i < 3) fa[n][i] = rd128(ad[(i & 2) + kk] + (i & 1) * 2048);
            else if (i == 3) fa[n][3] = rd128(ad[2 + kk] + 2048);
            else if (i == 4) fs[0] = rd128(ad[4 + kk]);
            else if (i == 8) fs[1] = rd128(ad[4 + kk] + 2048);
            else if (i == 6 || i == 10 || i == 14 || i == 18) fv[n][(i - 6) >> 2] = wfrag(8, (i - 6) >> 2, kk * 6144);
            else if (i == 12 || i == 16 || i == 20 || i == 24) fw[(i - 12) >> 2] = wfrag(6, (i - 12) >> 2, kk * 6144);
            if (kk == 1 && H < 14 && i >= 25 && i < 30) {
#pragma unroll
              for (int k = 2 * (i - 25); k < 2 * (i - 25) + 2; ++k) { ad[k] += ((H >> 1) % 3 == 2) ? -2 * STG : STG; asm volatile("" : "+v"(ad[k])); }
            }
          } else {
            if (i >= 5 && i < 13) { const int k = i - 5; *(LAS f4*)(smem + ((k & 2) ? ho ^ 2048u : ho) + (k >> 2) * 4096 + (k & 1) * 1024) = as[k >> 2][k & 3]; }
            if (i == 17) lds_post(smem + L_CNT + 16 + q * 4);
          }
          SB0();
        }
      };
      STAMP(2);
      waitv<63>(); BAR();
      STAMP(3);
      fs[0] = rd128(ad[4]);
#pragma unroll
      for (int i = 0; i < 4; ++i) fa[0][i] = rd128(ad[i & 2] + (i & 1) * 2048);
      fs[1] = rd128(ad[4] + 2048);
#pragma unroll
      for (int i = 0; i < 4; ++i) fw[i] = wfrag(6, i, 0);
#pragma unroll
      for (int i = 0; i < 4; ++i) fv[0][i] = wfrag(8, i, 0);
      step(IC<0>{}); step(IC<1>{}); step(IC<2>{}); step(IC<3>{}); step(IC<4>{}); step(IC<5>{}); step(IC<6>{});
      STAMP(4);
      step(IC<7>{}); step(IC<8>{}); step(IC<9>{}); step(IC<10>{}); step(IC<11>{}); step(IC<12>{}); FSTAMP(1); step(IC<13>{}); FSTAMP(2); step(IC<14>{}); FSTAMP(14); step(IC<15>{});
    }
    STAMP(5);
    LANEVARS()
    lds_wait(smem + L_CNT + 84, 1u);
    {
      auto glu_frag = [&](const f4 (&ac)[4], const f4 (&cs)[4], f2 st) -> u4 {
        const f2 nm = sp(opq(st.y)), a2 = sp(st.x);
        unsigned o[4];
#pragma unroll
        for (int i = 0; i < 2; ++i) {
          f2 g0 = fma2(nm, lo2(cs[i]), lo2(ac[i])), g1 = fma2(nm, hi2(cs[i]), hi2(ac[i]));
          f2 u0 = fma2(nm, lo2(cs[2 + i]), lo2(ac[2 + i])) * a2, u1 = fma2(nm, hi2(cs[2 + i]), hi2(ac[2 + i])) * a2;
          g0 = f2{fmaxf(g0.x, 0.f), fmaxf(g0.y, 0.f)} * u0;
          g1 = f2{fmaxf(g1.x, 0.f), fmaxf(g1.y, 0.f)} * u1;
          o[2 * i] = pk_bf(g0.x, g0.y); o[2 * i + 1] = pk_bf(g1.x, g1.y);
        }
        return u4{o[0], o[1], o[2], o[3]};
      };
      f4 cs[4], cv[4]; f2 st[4];
#pragma unroll
      for (int i = 0; i < 4; ++i) {
        cs[i] = *(LAS f4*)(smem + L_CS + ((i >> 1) * 96 + wn * 32 + (i & 1) * 16 + g * 4) * 4);
        st[i] = *(LAS f2*)(smem + L_PR + (wm * 64 + r16) * 8 + ((i * 128) ^ (wn * 256)));
      }
#pragma unroll
      for (int i = 0; i < 4; ++i) cv[i] = *(LAS f4*)(smem + L_CS + ((i >> 1) * 96 + 64 + (i & 1) * 16 + g * 4) * 4);
      const unsigned so = WS_HF + (unsigned)(t64 * 43 + fb0) * 4096u;
      const unsigned vo = (unsigned)(lane * 16 + wn * 2048);
#pragma unroll
      for (int m = 0; m < 4; ++m) { st128(glu_frag(ag[m], cs, st[m]), sWS, (m < 2 ? vo : vo ^ 2048u) + (m & 1) * 1024, so + wn * 4096); SB0(); }
#pragma unroll
      for (int m = 0; m < 2; ++m) { st128(glu_frag(av[m], cv, st[m]), sWS, vo + m * 1024, has3 ? so + 8192 : OOB); SB0(); }
    }
    STAMP(6);
    asm volatile("s_waitcnt vmcnt(0)" ::: "memory");
    __builtin_amdgcn_raw_buffer_store_b32(tag, sWS, lane == 0 ? WS_FLF + (unsigned)(t64 * 32 + cg * 2 + wn) * 128u : OOB, 0, 16);
    STAMP(7);
    u2 xv[4], xd[4], stg[4];
#pragma unroll
    for (int i = 0; i < 4; ++i) {
      const int tok = t2 * 64 + tokmap(q, 4 * i + g);
      xv[i] = __builtin_amdgcn_raw_buffer_load_b64(sX, (unsigned)(tok * 1024 + n64 * 128 + r16 * 8), 0, 0);
      stg[i] = __builtin_amdgcn_raw_buffer_load_b64(sWS, WS_STATS + (unsigned)tok * 8u, 0, 16);
    }
    const u2 dk = __builtin_amdgcn_raw_buffer_load_b64(mk_srd(p.dsk, DM * 2), (unsigned)(n64 * 128 + r16 * 8), 0, 0);
    lds_wait(smem + L_CNT + 32, 4u);
#pragma unroll
    for (int j = 6; j < 9; ++j) fill(j);
    const unsigned aD = q * 12288 + (4 * g + q4) * 128 + (((2 * g + (q4 >> 1)) & 3) * 32) + p4 * 8;
    auto bfrag = [&](int sl, int nt) -> bf8 { return cat(rdtr((aD ^ (nt * 32)) + slotoff(sl)), rdtr((aD ^ (nt * 32)) + slotoff(sl) + 2048)); };
    bf8 bc[4], bn[4];
    if (B01) {
#pragma unroll
      for (int nt = 0; nt < 4; ++nt) { bc[nt] = bfrag(0, nt); bn[nt] = bfrag(1, nt); }
      asm volatile("s_waitcnt lgkmcnt(0)" : "+v"(bc[0]), "+v"(bc[1]), "+v"(bc[2]), "+v"(bc[3]), "+v"(bn[0]), "+v"(bn[1]), "+v"(bn[2]), "+v"(bn[3])::"memory");
#pragma unroll
      for (int n = 0; n < 8; ++n) fillp(9 + (n >> 2), n >> 2, n & 3);
    }
    __builtin_amdgcn_s_sleep(PRE_F);
    pollflags(WS_FLF + (unsigned)(t2 * 32 + q * 8) * 128u, 8, 128u);
    STAMP(8);
    if (__builtin_expect(!__all((stg[0].y == tag) & (stg[1].y == tag) & (stg[2].y == tag) & (stg[3].y == tag)), 0)) {
#pragma unroll
      for (int i = 0; i < 4; ++i) stg[i] = __builtin_amdgcn_raw_buffer_load_b64(sWS, WS_STATS + (unsigned)(t2 * 64 + tokmap(q, 4 * i + g)) * 8u, 0, 16);
      asm volatile("" : "+v"(stg[0]), "+v"(stg[1]), "+v"(stg[2]), "+v"(stg[3]));
    }
    u4 af[11][4];
    auto lda = [&](int j, int m) {
      af[j][m] = __builtin_amdgcn_raw_buffer_load_b128(sWS, lane * 16 + m * 1024, (!NOA && 11 * q + kf(j) < 43) ? WS_HF + (unsigned)(t2 * 43 + 11 * q + kf(j)) * 4096u : OOB, AUXA);
    };
#pragma unroll
    for (int n = 0; n < WIN0 * 4; ++n) lda(n >> 2, n & 3);
    SB0();
    {
      const f4 ds = unpk(dk);
#pragma unroll
      for (int i = 0; i < 4; ++i) {
        const float mean = bf_lo(stg[i].x), rs = __builtin_amdgcn_rcpf(bf_hi(stg[i].x));
        const f4 xs = unpk(xv[i]);
        float d[4];
#pragma unroll
        for (int j = 0; j < 4; ++j) d[j] = rbf(rbf(xs[j] - mean) * rs) * ds[j];
        xd[i] = u2{pk_bf(d[0], d[1]), pk_bf(d[2], d[3])};
      }
      asm volatile("" : "+v"(xd[0]), "+v"(xd[1]), "+v"(xd[2]), "+v"(xd[3]), "+v"(xv[0]), "+v"(xv[1]), "+v"(xv[2]), "+v"(xv[3]));
    }
    STAMP(9);
    SB0();
    {
      const unsigned pw = q * 12288 + r16 * 256 + ((g ^ r16) * 16);
#pragma unroll
      for (int nt = 0; nt < 4; ++nt) if (!B01) bc[nt] = bfrag(0, nt);
#pragma unroll
      for (int j = 0; j < 11; ++j) {
#pragma unroll
        for (int i = 0; i < 16; ++i) {
          acc2[i >> 2][i & 3] = mfma(bc[i >> 2], asb(af[j][i & 3]), acc2[i >> 2][i & 3]);
          if (j + 1 < 11 && i < 4 && !(B01 && j == 0)) bn[i] = bfrag(j + 1 < 9 ? j + 1 : j + 1 - 9, i);
          {
            constexpr int R = WINF - WIN0;
            if (j < R) { if (i & 1) { const int n = (WIN0 + 2 * j) * 4 + (i >> 1); lda(n >> 2, n & 3); } }
            else if (j + WINF < 11 && (i & 3) == 3) lda(j + WINF, i >> 2);
          }
          if (!B01 && j >= 1 && j <= 2 && (i & 3) == 2) fillp(8 + j, j - 1, i >> 2);
          if (j == 4 && i == 12) lds_post(smem + L_CNT + 64 + q * 4);
          if (j == 10 && i >= 4) { const int k = i - 4; *(LAS f4*)(smem + slotoff((k & 3) < 3 ? 6 + (k & 3) : 0) + (pw ^ ((k >> 2) * 64))) = acc2[k >> 2][k & 3]; }
          SB0();
        }
#pragma unroll
        for (int nt = 0; nt < 4; ++nt) bc[nt] = bn[nt];
      }
      STAMP(10);
#pragma unroll
      for (int k = 12; k < 16; ++k) *(LAS f4*)(smem + slotoff((k & 3) < 3 ? 6 + (k & 3) : 0) + (pw ^ ((k >> 2) * 64))) = acc2[k >> 2][k & 3];
    }
    waitv<63>();
    if (lane == 0) __hip_atomic_fetch_add((LAS unsigned*)(smem + L_CNT), 1u, __ATOMIC_RELAXED, __HIP_MEMORY_SCOPE_WORKGROUP);
    lds_wait(smem + L_CNT, 4u);
    STAMP(11);
    const unsigned pf0 = q == 0 ? slotoff(6) : q == 1 ? slotoff(7) : q == 2 ? slotoff(8) : slotoff(0);
    const unsigned ps0 = q == 0 ? slotoff(2) : q == 1 ? slotoff(3) : q == 2 ? slotoff(4) : slotoff(5);
    f4 G[4], O[4];
#pragma unroll
    for (int i = 0; i < 4; ++i) {
      const int r = 4 * i + g;
      LAS char* src = smem + pf0 + r * 256 + ((r16 ^ r) * 16);
      const f4 yf = (*(LAS f4*)(src) + *(LAS f4*)(src + 12288)) + (*(LAS f4*)(src + 24576) + *(LAS f4*)(src + 36864));
#pragma unroll
      for (int j = 0; j < 4; ++j) {
        const float f = rbf(yf[j]);
        G[i][j] = rbf(f * __builtin_amdgcn_rcpf(1.0f + __expf(-f)));
      }
      O[i] = unpk(xd[i]) * G[i] + unpk(xv[i]);
    }
    asm volatile("" : "+v"(G[0]), "+v"(G[1]), "+v"(G[2]), "+v"(G[3]), "+v"(O[0]), "+v"(O[1]), "+v"(O[2]), "+v"(O[3]));
    STAMP(12);
    BAR();
#pragma unroll
    for (int i = 0; i < 4; ++i) {
      const int r = 4 * i + g;
      LAS char* src = smem + ps0 + r * 256 + ((r16 ^ r) * 16);
      const f4 ys = (*(LAS f4*)(src) + *(LAS f4*)(src + 12288)) + (*(LAS f4*)(src + 24576) + *(LAS f4*)(src + 36864));
      *(f4*)(p.out + (size_t)(t2 * 64 + tokmap(q, r)) * DM + n64 * 64 + r16 * 4) = ys * G[i] + O[i];
    }
    STAMP(13);
    STAMP_OUT();
    asm volatile(".Ls5_end_a:");
    __builtin_amdgcn_endpgm();
  }
}
}

extern "C" void kernel_launch(void* const* d_in, const int* in_sizes, int n_in, void* d_out, int out_size, void* d_ws,
                              size_t ws_size, hipStream_t stream) {
  Params p;
  p.x = d_in[0]; p.lre = d_in[1]; p.lim = d_in[2]; p.bre = d_in[3]; p.bim = d_in[4]; p.cre = d_in[5]; p.cim = d_in[6];
  p.ldt = d_in[7]; p.dsk = d_in[8]; p.wg = d_in[10]; p.wu = d_in[11]; p.wd = d_in[12];
  p.out = (float*)d_out; p.ws = (char*)d_ws;
  s5_glu_kernel<<<256, 512, 0, stream>>>(p);
}
```

```cpp
#include <hip/hip_runtime.h>
#include <stdint.h>

namespace {
typedef __attribute__((ext_vector_type(2))) __bf16 bf2;
typedef __attribute__((ext_vector_type(4))) __bf16 bf4;
typedef __attribute__((ext_vector_type(8))) __bf16 bf8;
typedef __attribute__((ext_vector_type(2))) float f2;
typedef __attribute__((ext_vector_type(4))) float f4;
typedef __attribute__((ext_vector_type(2))) unsigned u2;
typedef __attribute__((ext_vector_type(4))) unsigned u4;
typedef __amdgpu_buffer_rsrc_t srd_t;
#define LAS __attribute__((address_space(3)))
#define DEVI __device__ __forceinline__
template <int N> struct IC { static constexpr int v = N; };

constexpr int SEQ = 2048, DM = 512, FF = 1376;
constexpr unsigned WS_EPOCH = 0;
constexpr unsigned WS_STATS = 4096;
constexpr unsigned WS_FLC = 24576;
constexpr unsigned WS_FLF = 524288;
constexpr unsigned WS_FLS = 655360;
constexpr unsigned WS_CSB = 57344;
constexpr unsigned WS_AGG = 65536;
constexpr unsigned WS_CSW = 327680;
constexpr unsigned WS_CT = 1u << 20;
constexpr unsigned WS_HS = 2u << 20;
constexpr unsigned WS_HF = 6u << 20;
constexpr unsigned WS_END = WS_HF + 32u * 43u * 4096u;
constexpr unsigned OOB = 0x40000000u;
constexpr unsigned STG = 49152, L_A = 0, L_SB = 16384, L_WG = 24576, L_WU = 36864;
constexpr unsigned L_RS = 147456;
constexpr unsigned L_NM = 147968;
constexpr unsigned L_PR = 148480;
constexpr unsigned L_CS = 149504;
constexpr unsigned L_CSB = 150272;
constexpr unsigned L_CNT = 150528;
constexpr unsigned L_JUNK = 150784;
constexpr unsigned L_CAR = 151040;
constexpr unsigned L_CP = 151552;
constexpr unsigned L_TR = 155648;
constexpr int LDS_BYTES = 163840;

struct Params {
  const void *x, *lre, *lim, *bre, *bim, *cre, *cim, *ldt, *dsk, *wg, *wu, *wd;
  float* out;
  char* ws;
};

DEVI srd_t mk_srd(const void* p, unsigned bytes) { return __builtin_amdgcn_make_buffer_rsrc((void*)p, 0, bytes, 0x00020000); }
DEVI void glds16(unsigned m0v, unsigned voff, srd_t srd, unsigned soff) {
  asm volatile("s_mov_b32 m0, %0\n\ts_nop 0\n\tbuffer_load_dwordx4 %1, %2, %3 offen lds" ::"s"(m0v), "v"(voff), "s"(srd), "s"(soff) : "memory");
}
DEVI void glds16_sc1(unsigned m0v, unsigned voff, srd_t srd, unsigned soff) {
  asm volatile("s_mov_b32 m0, %0\n\ts_nop 0\n\tbuffer_load_dwordx4 %1, %2, %3 offen sc1 lds" ::"s"(m0v), "v"(voff), "s"(srd), "s"(soff) : "memory");
}
DEVI void glds4(unsigned m0v, unsigned voff, srd_t srd, unsigned soff) {
  asm volatile("s_mov_b32 m0, %0\n\ts_nop 0\n\tbuffer_load_dword %1, %2, %3 offen lds" ::"s"(m0v), "v"(voff), "s"(srd), "s"(soff) : "memory");
}
DEVI void glds4_sc1(unsigned m0v, unsigned voff, srd_t srd, unsigned soff) {
  asm volatile("s_mov_b32 m0, %0\n\ts_nop 0\n\tbuffer_load_dword %1, %2, %3 offen sc1 lds" ::"s"(m0v), "v"(voff), "s"(srd), "s"(soff) : "memory");
}
template <int N> DEVI void waitv() { asm volatile("s_waitcnt vmcnt(%0) lgkmcnt(0)" ::"i"(N) : "memory"); }
#define BAR() do { asm volatile("" ::: "memory"); __builtin_amdgcn_s_barrier(); asm volatile("" ::: "memory"); } while (0)
#define SB0() __builtin_amdgcn_sched_barrier(0)
DEVI float bf_lo(unsigned u) { return __uint_as_float(u << 16); }
DEVI float bf_hi(unsigned u) { return __uint_as_float(u & 0xffff0000u); }
DEVI float rbf(float v) { return (float)(__bf16)v; }
DEVI unsigned pk_bf(float a, float b) { bf2 r = {(__bf16)a, (__bf16)b}; return __builtin_bit_cast(unsigned, r); }
DEVI f4 mfma(bf8 a, bf8 b, f4 c) { return __builtin_amdgcn_mfma_f32_16x16x32_bf16(a, b, c, 0, 0, 0); }
DEVI void st128(u4 v, srd_t srd, unsigned voff, unsigned soff) {
  __builtin_amdgcn_raw_buffer_store_b128(v, srd, voff, soff, 16);
  asm volatile("s_nop 1" ::"v"(v) : "memory");
}
DEVI bf8 asb(u4 v) { return __builtin_bit_cast(bf8, v); }
DEVI f2 lo2(f4 v) { return __builtin_shufflevector(v, v, 0, 1); }
DEVI f2 hi2(f4 v) { return __builtin_shufflevector(v, v, 2, 3); }
DEVI f2 fma2(f2 a, f2 b, f2 c) { return __builtin_elementwise_fma(a, b, c); }
DEVI f2 sp(float a) { return f2{a, a}; }
DEVI u2 swap16(unsigned a) { auto r = __builtin_amdgcn_permlane16_swap(a, a, false, false); return u2{r[0], r[1]}; }
DEVI u2 swap32(unsigned a) { auto r = __builtin_amdgcn_permlane32_swap(a, a, false, false); return u2{r[0], r[1]}; }
DEVI float hsum(float v) {
  const unsigned u = __float_as_uint(v);
  auto b = swap32(u);
  return __uint_as_float(b[0]) + __uint_as_float(b[1]);
}
DEVI float gsum(float v) {
  const unsigned u = __float_as_uint(v);
  auto a = swap16(u);
  return hsum(__uint_as_float(a[0]) + __uint_as_float(a[1]));
}
template <int CTRL> DEVI float dpp_add(float v) {
  return v + __uint_as_float(__builtin_amdgcn_update_dpp(0u, __float_as_uint(v), CTRL, 0xf, 0xf, true));
}
DEVI float wave_sum(float v) {
  v = dpp_add<0xB1>(v);
  v = dpp_add<0x4E>(v);
  v = dpp_add<0x141>(v);
  v = dpp_add<0x140>(v);
  return gsum(v);
}
DEVI void gather4(float v, float& g0, float& g1, float& g2, float& g3) {
  unsigned u = __float_as_uint(v);
  auto a = swap16(u);
  auto e = swap32(a[0]);
  auto o = swap32(a[1]);
  g0 = __uint_as_float(e[0]); g2 = __uint_as_float(e[1]); g1 = __uint_as_float(o[0]); g3 = __uint_as_float(o[1]);
}
struct cf { float r, i; };
DEVI float opq(float v) { asm("" : "+v"(v)); return v; }
DEVI cf cmul(cf a, cf b) { return {fmaf(a.r, b.r, opq(-a.i * b.i)), fmaf(a.r, b.i, a.i * b.r)}; }
DEVI cf csq(cf a) { return {fmaf(a.r, a.r, opq(-a.i * a.i)), opq(a.r + a.r) * a.i}; }
DEVI cf cfma(cf a, cf b, cf c) { return {fmaf(a.r, b.r, opq(fmaf(-a.i, b.i, c.r))), fmaf(a.r, b.i, fmaf(a.i, b.r, c.i))}; }
DEVI int tokmap(int m, int r) { return 16 * (r >> 2) + 8 * (r & 1) + 2 * m + ((r >> 1) & 1); }

constexpr unsigned SPIN_CAP = 1u << 18;

constexpr int WIN0 = 3, WINF = 5, WINS = 3;
DEVI void nap() { __builtin_amdgcn_s_sleep(4); asm volatile("" ::: "memory"); }
DEVI unsigned lds_ld(LAS char* a) { return __hip_atomic_load((LAS unsigned*)a, __ATOMIC_RELAXED, __HIP_MEMORY_SCOPE_WORKGROUP); }
DEVI void lds_st(LAS char* a, unsigned v) { __hip_atomic_store((LAS unsigned*)a, v, __ATOMIC_RELAXED, __HIP_MEMORY_SCOPE_WORKGROUP); }
DEVI cf sel4(int g, cf a, cf b, cf c, cf d) { return {g == 0 ? a.r : g == 1 ? b.r : g == 2 ? c.r : d.r, g == 0 ? a.i : g == 1 ? b.i : g == 2 ? c.i : d.i}; }
struct Ssm { cf lam, coef2, l8, l16, l64; };
DEVI f4 ssm_base(unsigned short plre, unsigned short plim, unsigned short pldt) {
  const float dt = rbf(expf(bf_lo(pldt))), a = bf_lo(plre), b = bf_lo(plim);
  const float e = expf(dt * a);
  float sn, cn; sincosf(dt * b, &sn, &cn);
  const float lr = e * cn, li = e * sn, nr = lr - 1.f, den = 2.0f / (a * a + b * b);
  return f4{lr, li, fmaf(nr, a, opq(li * b)) * den, fmaf(li, a, -nr * b) * den};
}

#if defined(__HIP_DEVICE_COMPILE__)
#define KARG __attribute__((address_space(4)))
#else
#define KARG
#endif
DEVI constexpr unsigned slotoff(int sl) { return (unsigned)(((sl / 3 + 2) % 3) * STG + (sl % 3) * 4096); }
#define LANEVARS() \
  int tid = threadIdx.x; asm volatile("" : "+v"(tid)); \
  const int lane = tid & 63, r16 = lane & 15, g = lane >> 4; \
  const unsigned q4 = r16 >> 2, p4 = lane & 3; \
  const unsigned vD = (unsigned)((lane >> 3) * 1024 + n64 * 128 + ((((lane & 7) >> 1) ^ g) * 32) + (lane & 1) * 16); \
  auto fillp = [&](int j, int sl, int i) { glds16(slotoff(sl) + q * 12288 + i * 1024, vD, sD, (unsigned)((11 * q + j) * 32 + 8 * i) * 1024u); }; \
  auto fill = [&](int j) { fillp(j, j, 0); fillp(j, j, 1); fillp(j, j, 2); fillp(j, j, 3); }; \
  auto pollflags = [&](unsigned base, int n, unsigned stride) { \
    unsigned a_ = lane < n ? base + lane * stride : OOB; \
    _Pragma("unroll 1") for (unsigned spins = 0; spins < SPIN_CAP; ++spins) { \
      const unsigned f = __builtin_amdgcn_raw_buffer_load_b32(sWS, a_, 0, 16); \
      if (f == tag) a_ = OOB; \
      if (__all(a_ == OOB)) break; \
      nap(); \
    } \
    asm volatile("" ::: "memory"); \
  };
#define PREAMBLE() \
  const KARG Params* kp = (const KARG Params*)(uintptr_t)__builtin_amdgcn_kernarg_segment_ptr(); asm volatile("" : "+s"(kp)); \
  const Params p = *kp; \
  int bid = blockIdx.x, w = w_; asm volatile("" : "+s"(bid), "+s"(w)); \
  const int q = w & 3, xcd = bid & 7, slot = bid >> 3; \
  const srd_t sWS = mk_srd(p.ws, WS_END); \
  const int tb = (xcd >> 2) * 8 + (slot >> 2), cg = (xcd & 3) * 4 + (slot & 3); \
  const int t2 = xcd * 4 + (slot >> 3), n64 = slot & 7; \
  const int wm = q & 1, wn = q >> 1, t64 = tb * 2 + wm; \
  const bool has3 = (cg & 3) < 3 && cg != 14; \
  const int fb0 = 11 * (cg >> 2) + 3 * (cg & 3) - (cg == 15); \
  const srd_t sX = mk_srd(p.x, SEQ * DM * 2); \
  const srd_t sD = mk_srd(p.wd, FF * DM * 2); \
  unsigned tag; \
  f4 acc2[4][4] = {};
DEVI bf8 cat(bf4 a, bf4 b) { return __builtin_shufflevector(a, b, 0, 1, 2, 3, 4, 5, 6, 7); }
DEVI void lds_wait(LAS char* a, unsigned n) { while (lds_ld(a) < n) __builtin_amdgcn_s_sleep(1); asm volatile("" ::: "memory"); }
DEVI void lds_add64(unsigned addr) {
  unsigned long long keep;
  asm volatile("s_waitcnt lgkmcnt(0)\n\ts_mov_b64 %0, exec\n\ts_mov_b64 exec, 1\n\tds_add_u32 %1, %2\n\ts_mov_b64 exec, %0" : "=&s"(keep) : "v"(addr), "v"(64u) : "memory");
}
template <unsigned N> DEVI void lds_spin(unsigned addr) {
  unsigned v, sv;
  asm volatile("1:\n\tds_read_b32 %0, %2\n\ts_waitcnt lgkmcnt(0)\n\tv_readfirstlane_b32 %1, %0\n\ts_nop 1\n\ts_cmp_ge_u32 %1, %3\n\ts_cbranch_scc0 1b"
               : "=&v"(v), "=&s"(sv) : "v"(addr), "i"(N) : "memory", "scc");
}
DEVI void lds_post(LAS char* a) { asm volatile("s_waitcnt lgkmcnt(0)" ::: "memory"); lds_st(a, 1u); }
DEVI f4 unpk(u2 v) { return f4{bf_lo(v.x), bf_hi(v.x), bf_lo(v.y), bf_hi(v.y)}; }

__global__ __launch_bounds__(512, 2) void s5_glu_kernel(Params p_) {
  __shared__ __attribute__((aligned(16))) char smem_[LDS_BYTES];
  LAS char* smem = (LAS char*)smem_;
  auto rd128 = [&](unsigned addr) -> bf8 { return *(LAS bf8*)(smem + addr); };
  auto rdtr = [&](unsigned addr) -> bf4 { return __builtin_amdgcn_ds_read_tr16_b64_v4bf16((LAS bf4*)(smem + addr)); };
  asm volatile(".Ls5_start:");
  const int w_ = __builtin_amdgcn_readfirstlane(threadIdx.x >> 6);
  {
    unsigned long long pc; unsigned len, back;
    asm volatile("s_getpc_b64 %0\n.Ls5_pf_here:\n\ts_mov_b32 %1, .Ls5_end_a-.Ls5_start\n\ts_max_i32 %1, %1, .Ls5_end_b-.Ls5_start\n\ts_mov_b32 %2, .Ls5_pf_here-.Ls5_start" : "=s"(pc), "=s"(len), "=s"(back)::"scc");
    const srd_t sI = mk_srd((const void*)(pc - back), w_ == 0 ? len : 0u);
    glds4(L_JUNK, (unsigned)(((threadIdx.x & 63) * 32 + (blockIdx.x >> 3)) * 128), sI, 0);
  }
  if (w_ >= 4) {
    PREAMBLE()
    LANEVARS()
    const unsigned ch = (unsigned)(cg * 32 + wn * 16 + r16);
#define NEXT(n) do { waitv<n>(); BAR(); } while (0)
    Ssm K; cf l512, pwg, S0, S1, wT;
    auto consts_a = [&]() {
      const f4 k0 = *(LAS f4*)(smem + L_CP + (wn * 16 + r16) * 16);
      K.lam = {k0[0], opq(k0[1])}; K.coef2 = {k0[2], opq(k0[3])};
      K.l8 = csq(csq(csq(K.lam))); K.l16 = csq(K.l8); K.l64 = csq(csq(K.l16));
      asm volatile("" : "+v"(K.lam.r), "+v"(K.lam.i), "+v"(K.coef2.r), "+v"(K.coef2.i), "+v"(K.l8.r), "+v"(K.l8.i), "+v"(K.l16.r), "+v"(K.l16.i), "+v"(K.l64.r), "+v"(K.l64.i));
    };
    auto consts_b = [&]() {
      const cf l32 = csq(K.l16), l48 = cmul(l32, K.l16), l128 = csq(K.l64), l256 = csq(l128), l384 = cmul(l256, l128);
      l512 = csq(l256); pwg = sel4(g, cf{1.f, 0.f}, l128, l256, l384); wT = sel4(g, l48, l32, K.l16, cf{1.f, 0.f});
      S0 = cmul(K.coef2, sel4(g, cf{1.f, 0.f}, K.l16, l32, l48)); S1 = cmul(K.l8, S0);
      asm volatile("" : "+v"(l512.r), "+v"(l512.i), "+v"(pwg.r), "+v"(pwg.i), "+v"(S0.r), "+v"(S0.i), "+v"(S1.r), "+v"(S1.i), "+v"(wT.r), "+v"(wT.i));
    };
    if (q < 3) {
      const srd_t sa = mk_srd(q == 0 ? p.x : q == 1 ? p.wg : p.wu, q == 0 ? SEQ * DM * 2 : DM * FF * 2);
      const srd_t sb = mk_srd(q == 0 ? p.x : p.bre, q == 0 ? SEQ * DM * 2 : DM * DM * 2), sc = mk_srd(q == 0 ? p.x : p.bim, q == 0 ? SEQ * DM * 2 : DM * DM * 2);
      const unsigned stepa = q == 0 ? 128u : 64u * FF * 2;
      unsigned ma = q == 0 ? L_A : q == 1 ? L_WG : L_WU, mb = q == 0 ? L_A + 6144u : L_SB + (q - 1) * 4096u;
      unsigned vo[6], vb[2];
      const unsigned da = q == 0 ? 65536u : 32u * FF * 2, la = q == 0 ? 8192u : 6144u, db = q == 0 ? 65536u : 0u, lb = q == 0 ? 8192u : 2048u;
#pragma unroll
      for (int i = 0; i < 2; ++i) {
        const int r = 8 * i + (lane >> 3);
        vb[i] = (unsigned)(q == 0 ? tb * 128 + tokmap(3, r) : cg * 32 + (q - 1) * 16 + r) * 1024u + (unsigned)(((lane & 7) ^ ((r >> 1) & 7)) * 16);
      }
      auto pc1 = [&](int i, int s) {
        if (i < 12) glds16((s % 3) * STG + ma + (i / 6) * la + (i % 6) * 1024, vo[i % 6], sa, stepa * s + (i / 6) * da);
        else glds16((s % 3) * STG + mb + ((i - 12) >> 1) * lb + (i & 1) * 1024, vb[i & 1], i < 14 ? sb : sc, 128u * s + ((i - 12) >> 1) * db);
      };
      pc1(12, 0); pc1(13, 0); pc1(14, 0); pc1(15, 0);
#pragma unroll
      for (int i = 0; i < 3; ++i) {
        const int idx = 64 * i + lane, k = idx / 12, c = idx - 12 * k, u = (c >> 1) ^ ((k >> 3) & 1);
        const unsigned vw = (!has3 && u >= 4) ? OOB : (unsigned)(k * (FF * 2) + (fb0 * 32 + 16 * u + 8 * (c & 1)) * 2);
        vo[i] = q == 0 ? vb[i & 1] - (3 - (i >> 1)) * 2048u : vw;
        vo[i + 3] = q == 0 ? vb[(i + 1) & 1] - (3 - ((i + 3) >> 1)) * 2048u : vw + 16u * FF * 2;
        pc1(i, 0); pc1(i + 3, 0); pc1(i + 6, 0); pc1(i + 9, 0);
      }
      auto eight = [&](int hf, int s) {
        asm volatile("" : "+s"(ma), "+s"(mb));
#pragma unroll
        for (int j = 0; j < 8; ++j) pc1(j < 6 ? hf * 6 + j : 12 + hf * 2 + (j - 6), s);
      };
      auto fills = [&](int sl0) {
#pragma unroll
        for (int i = 0; i < 16; ++i) glds16(slotoff(sl0) + q * 4096 + (i >> 2) * 12288 + (i & 3) * 1024, vD, sD, (unsigned)((11 * (i >> 2) + sl0 + q) * 32 + 8 * (i & 3)) * 1024u);
      };
      eight(0, 1);
      asm volatile("s_waitcnt vmcnt(8)" ::: "memory");
      BAR();
      eight(1, 1); eight(0, 2); NEXT(8);
      eight(1, 2); eight(0, 3); NEXT(8);
      eight(1, 3); eight(0, 4); eight(1, 4); NEXT(16);
      eight(0, 5); eight(1, 5); consts_a(); NEXT(16);
      eight(0, 6); eight(1, 6); consts_b(); NEXT(16);
      eight(0, 7); eight(1, 7); NEXT(16);
      asm volatile("s_waitcnt vmcnt(0)" ::: "memory");
      lds_add64(L_CNT + 8);
      fills(0);
      lds_wait(smem + L_CNT + 12, 256u);
      fills(3);
      tag = __builtin_amdgcn_readfirstlane(lds_ld(smem + L_CNT + 4));
    } else {
      {
        const unsigned c2 = (unsigned)(cg * 32 + (lane & 31)) * 2u;
        *(LAS f4*)(smem + L_CP + (lane & 31) * 16) = ssm_base(__builtin_amdgcn_raw_buffer_load_b16(mk_srd(p.lre, 1024), c2, 0, 0), __builtin_amdgcn_raw_buffer_load_b16(mk_srd(p.lim, 1024), c2, 0, 0),
                                                              __builtin_amdgcn_raw_buffer_load_b16(mk_srd(p.ldt, 1024), c2, 0, 0));
        asm volatile("" ::: "memory");
      }
      const int jx = xcd & 3, jn = jx == 3 ? 10 : 11, ji = ((slot & 3) ^ 3) * 8 + (slot >> 2), jmat = ji >= jn, jl = ji - jn * jmat, jkh = xcd >> 2;
      const int je = 6 - (jx & 1), jln = jl < je ? (11 * jx + 1) / 2 + jl : 22 + (11 * jx) / 2 + (jl - je);
      const bool jon = ji < 2 * jn;
      const int cn64 = bid & 7, cks = bid >> 3;
      u4 wj[16];
      {
        const srd_t sJ = mk_srd(jmat ? p.wu : p.wg, jon ? DM * FF * 2 : 0);
        const unsigned vj = (unsigned)((lane >> 3) * 5504 + (lane & 7) * 16), sj = (unsigned)(jln * 128), s0 = (jkh ? 128u : 64u) * 5504u, s1 = (jkh ? 192u : 0u) * 5504u;
#pragma unroll
        for (int i = 0; i < 8; ++i) wj[i] = __builtin_amdgcn_raw_buffer_load_b128(sJ, vj, sj + s0 + i * 8 * 5504, 0);
        BAR();
#pragma unroll
        for (int i = 0; i < 8; ++i) wj[8 + i] = __builtin_amdgcn_raw_buffer_load_b128(sJ, vj, sj + s1 + i * 8 * 5504, 0);
        BAR();
      }
      tag = __builtin_amdgcn_readfirstlane(lds_ld(smem + L_CNT + 4));
      consts_a();
      BAR();
      {
        u4 sel;
#pragma unroll
        for (int i = 0; i < 4; ++i) sel[i] = (r16 >> 1) == i ? ((r16 & 1) ? 0x3f800000u : 0x00003f80u) : 0u;
        f4 d = {0.f, 0.f, 0.f, 0.f};
        asm volatile("" : "+v"(sel) :: "memory");
#pragma unroll
        for (int i = 0; i < 8; ++i) d = mfma(asb(wj[i]), asb(sel), d);
        asm volatile("" : "+v"(d));
        BAR();
#pragma unroll
        for (int i = 8; i < 16; ++i) d = mfma(asb(wj[i]), asb(sel), d);
        const unsigned so = jon ? WS_CSW + (unsigned)((jmat * 2 + jkh) * 2752 + jln * 64) * 8u : OOB;
#pragma unroll
        for (int j = 0; j < 4; ++j) __builtin_amdgcn_raw_buffer_store_b64(u2{__float_as_uint(hsum(d[j])), tag}, sWS, (lane & 0x28) == 0 ? (unsigned)(8 * (4 * g + j) + r16) * 8u : OOB, so, 16);
      }
      BAR();
      const unsigned gb = WS_STATS + (unsigned)(tb * 128 + 2 * lane) * 8u, gc = WS_CSB + (unsigned)(((lane >> 4) & 1) * 512 + cg * 32 + 2 * r16) * 8u;
      u4 g0 = __builtin_amdgcn_raw_buffer_load_b128(sWS, gb, 0, 16), g1 = __builtin_amdgcn_raw_buffer_load_b128(sWS, gc, 0, 16);
      consts_b();
      BAR();
#pragma unroll 1
      for (unsigned spins = 0; spins < SPIN_CAP && !__all((g0.y == tag) & (g0.w == tag) & (g1.y == tag) & (g1.w == tag)); ++spins) {
        nap();
        g0 = __builtin_amdgcn_raw_buffer_load_b128(sWS, gb, 0, 16); g1 = __builtin_amdgcn_raw_buffer_load_b128(sWS, gc, 0, 16);
        asm volatile("" : "+v"(g0), "+v"(g1));
      }
#pragma unroll
      for (int s = 0; s < 2; ++s) {
        const unsigned v = s ? g0.z : g0.x;
        const int rho = (lane & 32) * 2 + (lane & 3) * 16 + ((lane >> 3) & 3) * 4 + 2 * s + ((lane >> 2) & 1);
        const float rs = __builtin_amdgcn_rcpf(bf_hi(v)), nm = -bf_lo(v);
        *(LAS float*)(smem + L_RS + rho * 4) = rs;
        *(LAS float*)(smem + L_NM + rho * 4) = rs * nm;
        *(LAS float*)(smem + L_PR + rho * 4) = nm;
      }
      *(LAS u2*)(smem + L_CSB + (lane & 31) * 8) = u2{g1.x, g1.z};
      nap();
      glds4(L_JUNK, lane < 43 ? (unsigned)(bid * 43 + lane) * 128u : OOB, sD, 0);
#pragma unroll
      for (int i = 0; i < 4; ++i) {
        const int r = 8 * (i & 1) + (lane >> 3);
        glds16(L_TR + i * 1024, (unsigned)(r * 1024 + ((lane & 7) ^ ((r >> 1) & 7)) * 16), mk_srd((cks & 2) ? p.cim : p.cre, DM * DM * 2), (unsigned)((cn64 * 64 + (cks & 1) * 32 + (i >> 1) * 16) * 1024 + (cks >> 2) * 128));
      }
      glds4_sc1(L_JUNK, ((lane & 32) ? WS_FLS : WS_FLF) + (unsigned)(t2 * 32 + (lane & 31)) * 128u, sWS, 0);
      glds4_sc1(L_JUNK, (lane >> 2) < tb ? WS_AGG + (unsigned)((lane >> 2) * 8192 + cg * 512 + (lane & 3) * 128) : OOB, sWS, 0);
      NEXT(63);
      u4 gw[8];
      const unsigned ga = lane < (has3 ? 48 : 32) ? WS_CSW + (unsigned)(fb0 * 32 + 2 * lane) * 8u : OOB;
#pragma unroll
      for (int i = 0; i < 8; ++i) gw[i] = __builtin_amdgcn_raw_buffer_load_b128(sWS, ga, i * 1376 * 8, 16);
#pragma unroll 1
      for (unsigned spins = 0; spins < SPIN_CAP; ++spins) {
        bool ok = true;
#pragma unroll
        for (int i = 0; i < 8; ++i) ok &= (gw[i].y == tag) & (gw[i].w == tag);
        if (__builtin_expect(__all(ok | (ga == OOB)), 1)) break;
        nap();
#pragma unroll
        for (int i = 0; i < 8; ++i) gw[i] = __builtin_amdgcn_raw_buffer_load_b128(sWS, ga, i * 1376 * 8, 16);
      }
      if (lane < 48) {
#pragma unroll
        for (int m = 0; m < 2; ++m)
          *(LAS f2*)(smem + L_CS + m * 384 + lane * 8) = f2{(__uint_as_float(gw[4 * m].x) + __uint_as_float(gw[4 * m + 1].x)) + (__uint_as_float(gw[4 * m + 2].x) + __uint_as_float(gw[4 * m + 3].x)),
                                                            (__uint_as_float(gw[4 * m].z) + __uint_as_float(gw[4 * m + 1].z)) + (__uint_as_float(gw[4 * m + 2].z) + __uint_as_float(gw[4 * m + 3].z))};
      }
      lds_post(smem + L_CNT + 84);
      asm volatile("" ::: "memory");
#pragma unroll
      for (int i = 0; i < 4; ++i)
        st128(__builtin_bit_cast(u4, rd128(L_TR + (i >> 1) * 2048 + ((unsigned)(r16 * 128 + ((g ^ (r16 >> 1)) * 16)) ^ ((i & 1) * 64)))), sWS, lane * 16,
              WS_CT + (unsigned)(((cn64 * 32 + (cks >> 2) * 4 + (i & 1) * 2 + ((cks >> 1) & 1)) * 4 + (cks & 1) * 2 + (i >> 1)) * 1024));
      waitv<63>();
      lds_st(smem + L_CNT + 88, 1u);
    }
    u4 cfr[8][4];
    auto ldC = [&](int j) {
#pragma unroll
      for (int nt = 0; nt < 4; ++nt) cfr[j][nt] = __builtin_amdgcn_raw_buffer_load_b128(sWS, lane * 16 + nt * 1024, WS_CT + (unsigned)(n64 * 32 + q * 8 + j) * 4096u, 16);
    };
    f4 rs[4], br[4], bi[4];
    {
      const float csr = *(LAS float*)(smem + L_CSB + (wn * 16 + r16) * 4), csi = opq(*(LAS float*)(smem + L_CSB + (32 + wn * 16 + r16) * 4));
#pragma unroll
      for (int m = 0; m < 4; ++m) {
        rs[m] = *(LAS f4*)(smem + L_RS + (wm * 64 + 16 * m + 4 * g) * 4);
        const f4 t = *(LAS f4*)(smem + L_NM + (wm * 64 + 16 * m + 4 * g) * 4);
        br[m] = t * csr; bi[m] = t * csi;
        asm volatile("" : "+v"(br[m]), "+v"(bi[m]), "+v"(rs[m]));
      }
    }
    __builtin_amdgcn_s_setprio(3);
    f2 vr[8], vi[8];
    const f2 lr2 = sp(K.lam.r), li2 = sp(K.lam.i);
    lds_wait(smem + L_CNT + 16 + q * 4, 1u);
#pragma unroll
    for (int m = 0; m < 4; ++m) {
      const f4 xr = *(LAS f4*)(smem + STG + q * 12288 + m * 1024 + lane * 16), xi = *(LAS f4*)(smem + STG + q * 12288 + 4096 + m * 1024 + lane * 16);
      vr[2 * m] = fma2(lo2(rs[m]), lo2(xr), lo2(br[m])); vr[2 * m + 1] = fma2(hi2(rs[m]), hi2(xr), hi2(br[m]));
      vi[2 * m] = fma2(lo2(rs[m]), lo2(xi), lo2(bi[m])); vi[2 * m + 1] = fma2(hi2(rs[m]), hi2(xi), hi2(bi[m]));
    }
    f2 hr = vr[0], hi = vi[0];
#pragma unroll
    for (int k = 1; k < 8; ++k) {
      const f2 nr = fma2(lr2, hr, fma2(-li2, hi, vr[k])), ni = fma2(lr2, hi, fma2(li2, hr, vi[k]));
      hr = nr; hi = ni;
    }
    const cf runA = {hr.x, hi.x};
    const cf agg = cfma(K.l8, runA, cf{hr.y, hi.y});
    cf T = cmul(wT, agg);
    T = {gsum(T.r), gsum(T.i)};
    LAS char* car = smem + L_CAR + wn * 256 + r16 * 8;
    if (wm == 0) {
      *(LAS f2*)car = f2{T.r, T.i};
      lds_post(smem + L_CNT + 48 + wn * 4);
      asm volatile("s_waitcnt vmcnt(0)" ::: "memory");
    } else {
      lds_wait(smem + L_CNT + 48 + wn * 4, 1u);
      const f2 t0 = *(LAS f2*)car;
      const cf W = cfma(K.l64, cf{t0.x, opq(t0.y)}, T);
      st128(u4{__float_as_uint(W.r), tag, __float_as_uint(W.i), tag}, sWS, g == 0 ? ch * 16u : OOB, WS_AGG + (unsigned)tb * 8192u);
      asm volatile("s_waitcnt vmcnt(1)" ::: "memory");
    }
    if (lane == 0) __hip_atomic_fetch_add((LAS unsigned*)(smem + L_CNT + 32), 1u, __ATOMIC_RELAXED, __HIP_MEMORY_SCOPE_WORKGROUP);
    __builtin_amdgcn_raw_buffer_store_b32(tag, sWS, (q == 3 && lane == 0) ? WS_FLC + (unsigned)((bid & 7) * 32 + (bid >> 3)) * 4u : OOB, 0, 16);
    asm volatile("" ::: "memory");
    __builtin_amdgcn_s_setprio(0);
    const f2 cr2 = sp(K.coef2.r), ci2 = sp(K.coef2.i);
#pragma unroll
    for (int k = 0; k < 8; ++k) {
      const f2 a = fma2(cr2, vr[k], -(ci2 * vi[k])), b = -fma2(cr2, vi[k], ci2 * vr[k]);
      vr[k] = a; vi[k] = b;
      asm volatile("" : "+v"(vr[k]), "+v"(vi[k])::"memory");
    }
    cf E0, E1;
    {
      cf A0, A1, A2, A3;
      gather4(agg.r, A0.r, A1.r, A2.r, A3.r);
      gather4(agg.i, A0.i, A1.i, A2.i, A3.i);
      const cf P2 = cfma(K.l16, A0, A1), P3 = cfma(K.l16, P2, A2);
      E0 = cmul(K.coef2, sel4(g, cf{0.f, 0.f}, A0, P2, P3)); E1 = cfma(K.l8, E0, cmul(K.coef2, runA));
      asm volatile("" : "+v"(E0.r), "+v"(E0.i), "+v"(E1.r), "+v"(E1.i));
    }
    __builtin_amdgcn_s_setprio(3);
    cf carry;
    if (wm == 0) {
      u4 Xg[4]; unsigned ad[4];
#pragma unroll
      for (int i = 0; i < 4; ++i) { const int src = tb - 1 - (4 * i + g); ad[i] = src >= 0 ? WS_AGG + (unsigned)src * 8192u + ch * 16u : OOB; }
      unsigned todo = 0xfu;
#pragma unroll 1
      for (unsigned spins = 0; todo && spins < SPIN_CAP; ++spins) {
#pragma unroll
        for (int i = 0; i < 4; ++i) if (todo >> i & 1) Xg[i] = __builtin_amdgcn_raw_buffer_load_b128(sWS, ad[i], 0, 16);
        unsigned left = 0;
#pragma unroll
        for (int i = 0; i < 4; ++i) left |= __all((ad[i] == OOB) | ((Xg[i].y == tag) & (Xg[i].w == tag))) ? 0u : 1u << i;
        todo = left;
        if (todo) nap();
      }
      cf c = {__uint_as_float(Xg[3].x), __uint_as_float(Xg[3].z)};
#pragma unroll
      for (int i = 2; i >= 0; --i) c = cfma(l512, c, cf{__uint_as_float(Xg[i].x), __uint_as_float(Xg[i].z)});
      c = cmul(c, pwg);
      carry = {gsum(c.r), gsum(c.i)};
      const cf c1 = cfma(K.l64, carry, T);
      *(LAS f2*)(car + 128) = f2{c1.r, c1.i};
      lds_post(smem + L_CNT + 56 + wn * 4);
      if (tb == 15 && cg == 0 && wn == 0 && lane == 0) __builtin_amdgcn_raw_buffer_store_b32(tag, sWS, WS_EPOCH, 0, 16);
    } else {
      lds_wait(smem + L_CNT + 56 + wn * 4, 1u);
      const f2 c1 = *(LAS f2*)(car + 128);
      carry = {c1.x, opq(c1.y)};
    }
    {
      lds_wait(smem + L_CNT + 88, 1u);
      carry = {opq(carry.r), opq(carry.i)};
      const cf h0 = cfma(S0, carry, E0), h1 = cfma(S1, carry, E1);
      hr = f2{h0.r, h1.r}; hi = f2{-h0.i, -h1.i};
      const unsigned so = WS_HS + (unsigned)(t64 * 32 + cg * 2) * 4096u + (unsigned)wn * 512u;
      const unsigned vo = (unsigned)((g >> 1) * 4096 + (lane & 31) * 16);
      const unsigned wa = L_TR + q * 2048 + r16 * 32 + g * 8, qa = L_TR + q * 2048 + (8 * g + q4) * 32 + p4 * 8;
      bf8 o;
#pragma unroll
      for (int m = 0; m < 4; ++m) {
        unsigned pr[2], pi[2];
#pragma unroll
        for (int j = 0; j < 2; ++j) {
          const int k = 2 * m + j;
          const f2 nr = fma2(lr2, hr, fma2(li2, hi, vr[k])), ni = fma2(lr2, hi, fma2(-li2, hr, vi[k]));
          hr = nr; hi = ni;
          pr[j] = pk_bf(hr.x, hr.y); pi[j] = pk_bf(hi.x, hi.y);
        }
        if (m > 0) st128(__builtin_bit_cast(u4, o), sWS, vo + (m - 1) * 1024, so);
        *(LAS u2*)(smem + wa + (m & 1) * 1024) = u2{pr[0], pr[1]};
        *(LAS u2*)(smem + wa + (m & 1) * 1024 + 512) = u2{pi[0], pi[1]};
        asm volatile("" ::: "memory");
        o = cat(rdtr(qa + (m & 1) * 1024), rdtr(qa + (m & 1) * 1024 + 128));
        asm volatile("" ::: "memory");
      }
      st128(__builtin_bit_cast(u4, o), sWS, vo + 3 * 1024, so);
    }
    asm volatile("s_waitcnt vmcnt(0)" ::: "memory");
    __builtin_amdgcn_raw_buffer_store_b32(tag, sWS, lane == 0 ? WS_FLS + (unsigned)(t64 * 32 + cg * 2 + wn) * 128u : OOB, 0, 16);
    __builtin_amdgcn_s_setprio(0);
    pollflags(WS_FLC + (unsigned)(n64 * 128 + q * 32), 8, 4u);
#pragma unroll
    for (int j = 0; j < 8; ++j) ldC(j);
    nap();
    pollflags(WS_FLS + (unsigned)(t2 * 32 + q * 8) * 128u, 8, 128u);
    u4 af[8][4];
    const unsigned pw = q * 12288 + r16 * 256 + ((g ^ r16) * 16);
    auto lda = [&](int j, int m) {
      af[j][m] = __builtin_amdgcn_raw_buffer_load_b128(sWS, lane * 16 + m * 1024, WS_HS + (unsigned)(t2 * 32 + q * 8 + j) * 4096u, 16);
    };
#pragma unroll
    for (int n = 0; n < WINS * 4; ++n) lda(n >> 2, n & 3);
    SB0();
#pragma unroll
    for (int j = 0; j < 8; ++j) {
      if (j == 7) lds_wait(smem + L_CNT + 64 + q * 4, 1u);
#pragma unroll
      for (int i = 0; i < 16; ++i) {
        if (j == 7 && i >= 5) { const int k = i - 5; *(LAS f4*)(smem + slotoff(2 + (k & 3)) + (pw ^ ((k >> 2) * 64))) = acc2[k >> 2][k & 3]; }
        acc2[i >> 2][i & 3] = mfma(asb(cfr[j][i >> 2]), asb(af[j][i & 3]), acc2[i >> 2][i & 3]);
        if (j >= 1) { const int n = WINS * 4 + (j - 1) * 8 + (i >> 1); if ((i & 1) && n < 32) lda(n >> 2, n & 3); }
        SB0();
      }
    }
#pragma unroll
    for (int k = 11; k < 16; ++k) *(LAS f4*)(smem + slotoff(2 + (k & 3)) + (pw ^ ((k >> 2) * 64))) = acc2[k >> 2][k & 3];
    waitv<63>();
    BAR();
    asm volatile(".Ls5_end_b:");
    __builtin_amdgcn_endpgm();
  } else {
    PREAMBLE()
    f4 ag[4][4] = {}, av[2][4] = {};
    {
      LANEVARS()
      {
        const int brow = (tb >> 3) * 512 + cg * 32 + (tb & 7) * 4 + q, xr = tb * 128 + cg * 8 + 2 * q;
        u4 xa[2];
#pragma unroll
        for (int i = 0; i < 2; ++i) xa[i] = __builtin_amdgcn_raw_buffer_load_b128(sX, lane * 16 + i * 1024, (unsigned)xr * 1024u, 0);
        const unsigned pfw = __builtin_amdgcn_raw_buffer_load_b32(mk_srd(q < 2 ? p.wg : p.wu, DM * FF * 2), lane < 6 ? (unsigned)((bid * 2 + (q & 1)) * 6 + lane) * 128u : OOB, 0, 0);
        const u4 br = __builtin_amdgcn_raw_buffer_load_b128(mk_srd((brow >> 9) ? p.bim : p.bre, DM * DM * 2), lane * 16, (unsigned)(brow & 511) * 1024u, 0);
        const unsigned ep = __builtin_amdgcn_raw_buffer_load_b32(sWS, 0, WS_EPOCH, 16);
        SB0();
        float mean[2], sd[2];
#pragma unroll
        for (int i = 0; i < 2; ++i) {
          float v[8];
#pragma unroll
          for (int j = 0; j < 4; ++j) { v[2 * j] = bf_lo(xa[i][j]); v[2 * j + 1] = bf_hi(xa[i][j]); }
          mean[i] = wave_sum(((v[0] + v[1]) + (v[2] + v[3])) + ((v[4] + v[5]) + (v[6] + v[7]))) * (1.0f / DM);
          float qq = 0.f;
#pragma unroll
          for (int j = 0; j < 8; ++j) { const float dd = v[j] - mean[i]; qq = fmaf(dd, dd, qq); }
          sd[i] = __builtin_amdgcn_sqrtf(rbf(rbf(wave_sum(qq) * (1.0f / DM)) + 1e-5f));
        }
        asm volatile("" ::"v"(pfw));
        tag = __builtin_amdgcn_readfirstlane(ep) + 1u;
        if (tid < 32) lds_st(smem + L_CNT + tid * 4, tid == 1 ? tag : 0u);
        st128(u4{pk_bf(mean[0], sd[0]), tag, pk_bf(mean[1], sd[1]), tag}, sWS, lane == 0 ? WS_STATS + (unsigned)xr * 8u : OOB, 0);
        float s = 0.f;
#pragma unroll
        for (int j = 0; j < 4; ++j) s += bf_lo(br[j]) + bf_hi(br[j]);
        s = wave_sum(s);
        __builtin_amdgcn_raw_buffer_store_b64(u2{__float_as_uint(s), tag}, sWS, lane == 0 ? WS_CSB + (unsigned)brow * 8u : OOB, 0, 16);
      }
      f4 as[2][4] = {};
      unsigned ad[10];
      const unsigned aA0 = (unsigned)(r16 * 128 + ((g ^ (r16 >> 1)) * 16));
      ad[0] = (aA0 + wm * 8192) ^ (wn * 4096); ad[1] = ad[0] ^ 64u; ad[2] = ad[0] ^ 4096u; ad[3] = ad[2] ^ 64u;
      ad[4] = L_SB + aA0 + wn * 4096; ad[5] = ad[4] ^ 64u;
      ad[8] = L_WG + (unsigned)((8 * g + q4) * 192 + 8 * p4 + (g & 1) * 32) + 128u; ad[9] = ad[8] ^ 32u;
      ad[6] = ad[8] - 128u + wn * 64u; ad[7] = ad[6] ^ 32u;
#pragma unroll
      for (int i = 0; i < 10; ++i) asm volatile("" : "+v"(ad[i]));
      bf8 fa[2][4], fs[2], fw[4], fv[2][4];
      auto wfrag = [&](int b, int i, unsigned off) -> bf8 {
        const unsigned a = ad[b + (i & 1)] + (i >> 1) * 12288 + off;
        return cat(rdtr(a), rdtr(a + 768));
      };
      auto step = [&](auto H_) {
        constexpr int H = decltype(H_)::v, c = H & 1, n = c ^ 1;
        constexpr int kk = (H + 1) & 1;
        if ((H & 1) && H < 13) { waitv<63>(); BAR(); }
        if (H == 13) { lds_add64(L_CNT + 12); lds_spin<192>(L_CNT + 8); }
        if (H == 15) { lds_add64(L_CNT + 80); lds_spin<256>(L_CNT + 80); }
        unsigned ho = 0;
        if (H == 15) { int t_ = threadIdx.x; asm volatile("" : "+v"(t_)); ho = STG + q * 12288 + wn * 2048 + (t_ & 63) * 16; }
        SB0();
#pragma unroll
        for (int i = 0; i < 32; ++i) {
          if (i < 8) as[i >> 2][i & 3] = mfma(fa[c][i & 3], fs[i >> 2], as[i >> 2][i & 3]);
          else if (i < 24) ag[i & 3][(i - 8) >> 2] = mfma(fw[(i - 8) >> 2], fa[c][i & 3], ag[i & 3][(i - 8) >> 2]);
          else av[i & 1][(i - 24) >> 1] = mfma(fv[c][(i - 24) >> 1], fa[c][i & 1], av[i & 1][(i - 24) >> 1]);
          if (H < 15) {
            if (i < 3) fa[n][i] = rd128(ad[(i & 2) + kk] + (i & 1) * 2048);
            else if (i == 3) fa[n][3] = rd128(ad[2 + kk] + 2048);
            else if (i == 4) fs[0] = rd128(ad[4 + kk]);
            else if (i == 8) fs[1] = rd128(ad[4 + kk] + 2048);
            else if (i == 6 || i == 10 || i == 14 || i == 18) fv[n][(i - 6) >> 2] = wfrag(8, (i - 6) >> 2, kk * 6144);
            else if (i == 12 || i == 16 || i == 20 || i == 24) fw[(i - 12) >> 2] = wfrag(6, (i - 12) >> 2, kk * 6144);
            if (kk == 1 && H < 14 && i >= 25 && i < 30) {
#pragma unroll
              for (int k = 2 * (i - 25); k < 2 * (i - 25) + 2; ++k) { ad[k] += ((H >> 1) % 3 == 2) ? -2 * STG : STG; asm volatile("" : "+v"(ad[k])); }
            }
          } else {
            if (i >= 5 && i < 13) { const int k = i - 5; *(LAS f4*)(smem + ((k & 2) ? ho ^ 2048u : ho) + (k >> 2) * 4096 + (k & 1) * 1024) = as[k >> 2][k & 3]; }
            if (i == 17) lds_post(smem + L_CNT + 16 + q * 4);
          }
          SB0();
        }
      };
      waitv<63>(); BAR();
      fs[0] = rd128(ad[4]);
#pragma unroll
      for (int i = 0; i < 4; ++i) fa[0][i] = rd128(ad[i & 2] + (i & 1) * 2048);
      fs[1] = rd128(ad[4] + 2048);
#pragma unroll
      for (int i = 0; i < 4; ++i) fw[i] = wfrag(6, i, 0);
#pragma unroll
      for (int i = 0; i < 4; ++i) fv[0][i] = wfrag(8, i, 0);
      step(IC<0>{}); step(IC<1>{}); step(IC<2>{}); step(IC<3>{}); step(IC<4>{}); step(IC<5>{}); step(IC<6>{});
      step(IC<7>{}); step(IC<8>{}); step(IC<9>{}); step(IC<10>{}); step(IC<11>{}); step(IC<12>{}); step(IC<13>{}); step(IC<14>{}); step(IC<15>{});
    }
    LANEVARS()
    lds_wait(smem + L_CNT + 84, 1u);
    {
      auto glu_frag = [&](const f4 (&ac)[4], const f4 (&cs)[4], float st) -> u4 {
        const f2 nm = sp(st);
        unsigned o[4];
#pragma unroll
        for (int i = 0; i < 2; ++i) {
          f2 g0 = fma2(nm, lo2(cs[i]), lo2(ac[i])), g1 = fma2(nm, hi2(cs[i]), hi2(ac[i]));
          f2 u0 = fma2(nm, lo2(cs[2 + i]), lo2(ac[2 + i])), u1 = fma2(nm, hi2(cs[2 + i]), hi2(ac[2 + i]));
          g0 = f2{fmaxf(g0.x, 0.f), fmaxf(g0.y, 0.f)} * u0;
          g1 = f2{fmaxf(g1.x, 0.f), fmaxf(g1.y, 0.f)} * u1;
          o[2 * i] = pk_bf(g0.x, g0.y); o[2 * i + 1] = pk_bf(g1.x, g1.y);
        }
        return u4{o[0], o[1], o[2], o[3]};
      };
      f4 cs[4], cv[4]; float st[4];
#pragma unroll
      for (int i = 0; i < 4; ++i) {
        cs[i] = *(LAS f4*)(smem + L_CS + ((i >> 1) * 96 + wn * 32 + (i & 1) * 16 + g * 4) * 4);
        st[i] = opq(*(LAS float*)(smem + L_PR + (wm * 64 + r16) * 4 + ((i * 64) ^ (wn * 128))));
      }
#pragma unroll
      for (int i = 0; i < 4; ++i) cv[i] = *(LAS f4*)(smem + L_CS + ((i >> 1) * 96 + 64 + (i & 1) * 16 + g * 4) * 4);
      const unsigned so = WS_HF + (unsigned)(t64 * 43 + fb0) * 4096u;
      const unsigned vo = (unsigned)(lane * 16 + wn * 2048);
#pragma unroll
      for (int m = 0; m < 4; ++m) { st128(glu_frag(ag[m], cs, st[m]), sWS, (m < 2 ? vo : vo ^ 2048u) + (m & 1) * 1024, so + wn * 4096); SB0(); }
#pragma unroll
      for (int m = 0; m < 2; ++m) { st128(glu_frag(av[m], cv, st[m]), sWS, vo + m * 1024, has3 ? so + 8192 : OOB); SB0(); }
    }
    asm volatile("s_waitcnt vmcnt(0)" ::: "memory");
    __builtin_amdgcn_raw_buffer_store_b32(tag, sWS, lane == 0 ? WS_FLF + (unsigned)(t64 * 32 + cg * 2 + wn) * 128u : OOB, 0, 16);
    u2 xv[4], xd[4], stg[4]; float r2[4];
#pragma unroll
    for (int i = 0; i < 4; ++i) {
      const int tok = t2 * 64 + tokmap(q, 4 * i + g);
      xv[i] = __builtin_amdgcn_raw_buffer_load_b64(sX, (unsigned)(tok * 1024 + n64 * 128 + r16 * 8), 0, 0);
      stg[i] = __builtin_amdgcn_raw_buffer_load_b64(sWS, WS_STATS + (unsigned)tok * 8u, 0, 16);
    }
    const u2 dk = __builtin_amdgcn_raw_buffer_load_b64(mk_srd(p.dsk, DM * 2), (unsigned)(n64 * 128 + r16 * 8), 0, 0);
    lds_wait(smem + L_CNT + 32, 4u);
#pragma unroll
    for (int j = 6; j < 9; ++j) fill(j);
    const unsigned aD = q * 12288 + (4 * g + q4) * 128 + (((2 * g + (q4 >> 1)) & 3) * 32) + p4 * 8;
    auto bfrag = [&](int sl, int nt) -> bf8 { return cat(rdtr((aD ^ (nt * 32)) + slotoff(sl)), rdtr((aD ^ (nt * 32)) + slotoff(sl) + 2048)); };
    bf8 bc[4], bn[4];
    nap();
    pollflags(WS_FLF + (unsigned)(t2 * 32 + q * 8) * 128u, 8, 128u);
    if (__builtin_expect(!__all((stg[0].y == tag) & (stg[1].y == tag) & (stg[2].y == tag) & (stg[3].y == tag)), 0)) {
#pragma unroll
      for (int i = 0; i < 4; ++i) stg[i] = __builtin_amdgcn_raw_buffer_load_b64(sWS, WS_STATS + (unsigned)(t2 * 64 + tokmap(q, 4 * i + g)) * 8u, 0, 16);
      asm volatile("" : "+v"(stg[0]), "+v"(stg[1]), "+v"(stg[2]), "+v"(stg[3]));
    }
    u4 af[11][4];
    auto lda = [&](int j, int m) {
      af[j][m] = __builtin_amdgcn_raw_buffer_load_b128(sWS, lane * 16 + m * 1024, (j < 10 || q < 3) ? WS_HF + (unsigned)(t2 * 43 + 11 * q + j) * 4096u : OOB, 16);
    };
#pragma unroll
    for (int n = 0; n < WIN0 * 4; ++n) lda(n >> 2, n & 3);
    SB0();
    {
      const f4 ds = unpk(dk);
#pragma unroll
      for (int i = 0; i < 4; ++i) {
        const float mean = bf_lo(stg[i].x), rs = __builtin_amdgcn_rcpf(bf_hi(stg[i].x));
        const f4 xs = unpk(xv[i]);
        float d[4];
#pragma unroll
        for (int j = 0; j < 4; ++j) d[j] = rbf(rbf(xs[j] - mean) * rs) * ds[j];
        xd[i] = u2{pk_bf(d[0], d[1]), pk_bf(d[2], d[3])};
        r2[i] = rs * rs;
      }
      asm volatile("" : "+v"(xd[0]), "+v"(xd[1]), "+v"(xd[2]), "+v"(xd[3]), "+v"(xv[0]), "+v"(xv[1]), "+v"(xv[2]), "+v"(xv[3]));
    }
    SB0();
    {
      const unsigned pw = q * 12288 + r16 * 256 + ((g ^ r16) * 16);
#pragma unroll
      for (int nt = 0; nt < 4; ++nt) bc[nt] = bfrag(0, nt);
#pragma unroll
      for (int j = 0; j < 11; ++j) {
#pragma unroll
        for (int i = 0; i < 16; ++i) {
          acc2[i >> 2][i & 3] = mfma(bc[i >> 2], asb(af[j][i & 3]), acc2[i >> 2][i & 3]);
          if (j + 1 < 11 && i < 4) bn[i] = bfrag(j + 1 < 9 ? j + 1 : j + 1 - 9, i);
          {
            constexpr int R = WINF - WIN0;
            if (j < R) { if (i & 1) { const int n = (WIN0 + 2 * j) * 4 + (i >> 1); lda(n >> 2, n & 3); } }
            else if (j + WINF < 11 && (i & 3) == 3) lda(j + WINF, i >> 2);
          }
          if (j >= 1 && j <= 2 && (i & 3) == 2) fillp(8 + j, j - 1, i >> 2);
          if (j == 4 && i == 12) lds_post(smem + L_CNT + 64 + q * 4);
          if (j == 10 && i >= 4) { const int k = i - 4; *(LAS f4*)(smem + slotoff((k & 3) < 3 ? 6 + (k & 3) : 0) + (pw ^ ((k >> 2) * 64))) = acc2[k >> 2][k & 3]; }
          SB0();
        }
#pragma unroll
        for (int nt = 0; nt < 4; ++nt) bc[nt] = bn[nt];
      }
#pragma unroll
      for (int k = 12; k < 16; ++k) *(LAS f4*)(smem + slotoff((k & 3) < 3 ? 6 + (k & 3) : 0) + (pw ^ ((k >> 2) * 64))) = acc2[k >> 2][k & 3];
    }
    waitv<63>();
    if (lane == 0) __hip_atomic_fetch_add((LAS unsigned*)(smem + L_CNT), 1u, __ATOMIC_RELAXED, __HIP_MEMORY_SCOPE_WORKGROUP);
    lds_wait(smem + L_CNT, 4u);
    const unsigned pf0 = q == 0 ? slotoff(6) : q == 1 ? slotoff(7) : q == 2 ? slotoff(8) : slotoff(0);
    const unsigned ps0 = q == 0 ? slotoff(2) : q == 1 ? slotoff(3) : q == 2 ? slotoff(4) : slotoff(5);
    f4 G[4], O[4];
#pragma unroll
    for (int i = 0; i < 4; ++i) {
      const int r = 4 * i + g;
      LAS char* src = smem + pf0 + r * 256 + ((r16 ^ r) * 16);
      const f4 yf = (*(LAS f4*)(src) + *(LAS f4*)(src + 12288)) + (*(LAS f4*)(src + 24576) + *(LAS f4*)(src + 36864));
#pragma unroll
      for (int j = 0; j < 4; ++j) {
        const float f = rbf(yf[j] * r2[i]);
        G[i][j] = rbf(f * __builtin_amdgcn_rcpf(1.0f + __expf(-f)));
      }
      O[i] = unpk(xd[i]) * G[i] + unpk(xv[i]);
    }
    asm volatile("" : "+v"(G[0]), "+v"(G[1]), "+v"(G[2]), "+v"(G[3]), "+v"(O[0]), "+v"(O[1]), "+v"(O[2]), "+v"(O[3]));
    BAR();
#pragma unroll
    for (int i = 0; i < 4; ++i) {
      const int r = 4 * i + g;
      LAS char* src = smem + ps0 + r * 256 + ((r16 ^ r) * 16);
      const f4 ys = (*(LAS f4*)(src) + *(LAS f4*)(src + 12288)) + (*(LAS f4*)(src + 24576) + *(LAS f4*)(src + 36864));
      *(f4*)(p.out + (size_t)(t2 * 64 + tokmap(q, r)) * DM + n64 * 64 + r16 * 4) = ys * G[i] + O[i];
    }
    asm volatile(".Ls5_end_a:");
    __builtin_amdgcn_endpgm();
  }
}
}

extern "C" void kernel_launch(void* const* d_in, const int* in_sizes, int n_in, void* d_out, int out_size, void* d_ws,
                              size_t ws_size, hipStream_t stream) {
  Params p;
  p.x = d_in[0]; p.lre = d_in[1]; p.lim = d_in[2]; p.bre = d_in[3]; p.bim = d_in[4]; p.cre = d_in[5]; p.cim = d_in[6];
  p.ldt = d_in[7]; p.dsk = d_in[8]; p.wg = d_in[10]; p.wu = d_in[11]; p.wd = d_in[12];
  p.out = (float*)d_out; p.ws = (char*)d_ws;
  s5_glu_kernel<<<256, 512, 0, stream>>>(p);
}
```

```cpp
#include <hip/hip_runtime.h>
#include <stdint.h>

namespace {
typedef __attribute__((ext_vector_type(2))) __bf16 bf2;
typedef __attribute__((ext_vector_type(4))) __bf16 bf4;
typedef __attribute__((ext_vector_type(8))) __bf16 bf8;
typedef __attribute__((ext_vector_type(2))) float f2;
typedef __attribute__((ext_vector_type(4))) float f4;
typedef __attribute__((ext_vector_type(2))) unsigned u2;
typedef __attribute__((ext_vector_type(4))) unsigned u4;
typedef __amdgpu_buffer_rsrc_t srd_t;
#define LAS __attribute__((address_space(3)))
#define DEVI __device__ __forceinline__
template <int N> struct IC { static constexpr int v = N; };

constexpr int SEQ = 2048, DM = 512, FF = 1376;
constexpr unsigned WS_EPOCH = 0;
constexpr unsigned WS_STATS = 4096;
constexpr unsigned WS_FLC = 24576;
constexpr unsigned WS_FLF = 524288;
constexpr unsigned WS_FLS = 655360;
constexpr unsigned WS_CSB = 57344;
constexpr unsigned WS_AGG = 65536;
constexpr unsigned WS_CSW = 327680;
constexpr unsigned WS_CT = 1u << 20;
constexpr unsigned WS_HS = 2u << 20;
constexpr unsigned WS_HF = 6u << 20;
constexpr unsigned WS_END = WS_HF + 32u * 43u * 4096u;
constexpr unsigned OOB = 0x40000000u;
constexpr unsigned STG = 49152, L_A = 0, L_SB = 16384, L_WG = 24576, L_WU = 36864;
constexpr unsigned L_RS = 147456;
constexpr unsigned L_NM = 147968;
constexpr unsigned L_PR = 148480;
constexpr unsigned L_CS = 149504;
constexpr unsigned L_CSB = 150272;
constexpr unsigned L_CNT = 150528;
constexpr unsigned L_JUNK = 150784;
constexpr unsigned L_CAR = 151040;
constexpr unsigned L_CP = 151552;
constexpr unsigned L_TR = 155648;
constexpr int LDS_BYTES = 163840;

struct Params {
  const void *x, *lre, *lim, *bre, *bim, *cre, *cim, *ldt, *dsk, *wg, *wu, *wd;
  float* out;
  char* ws;
};

DEVI srd_t mk_srd(const void* p, unsigned bytes) { return __builtin_amdgcn_make_buffer_rsrc((void*)p, 0, bytes, 0x00020000); }
DEVI void glds16(unsigned m0v, unsigned voff, srd_t srd, unsigned soff) {
  asm volatile("s_mov_b32 m0, %0\n\ts_nop 0\n\tbuffer_load_dwordx4 %1, %2, %3 offen lds" ::"s"(m0v), "v"(voff), "s"(srd), "s"(soff) : "memory");
}
DEVI void glds16_sc1(unsigned m0v, unsigned voff, srd_t srd, unsigned soff) {
  asm volatile("s_mov_b32 m0, %0\n\ts_nop 0\n\tbuffer_load_dwordx4 %1, %2, %3 offen sc1 lds" ::"s"(m0v), "v"(voff), "s"(srd), "s"(soff) : "memory");
}
DEVI void glds4(unsigned m0v, unsigned voff, srd_t srd, unsigned soff) {
  asm volatile("s_mov_b32 m0, %0\n\ts_nop 0\n\tbuffer_load_dword %1, %2, %3 offen lds" ::"s"(m0v), "v"(voff), "s"(srd), "s"(soff) : "memory");
}
DEVI void glds4_sc1(unsigned m0v, unsigned voff, srd_t srd, unsigned soff) {
  asm volatile("s_mov_b32 m0, %0\n\ts_nop 0\n\tbuffer_load_dword %1, %2, %3 offen sc1 lds" ::"s"(m0v), "v"(voff), "s"(srd), "s"(soff) : "memory");
}
template <int N> DEVI void waitv() { asm volatile("s_waitcnt vmcnt(%0) lgkmcnt(0)" ::"i"(N) : "memory"); }
#define BAR() do { asm volatile("" ::: "memory"); __builtin_amdgcn_s_barrier(); asm volatile("" ::: "memory"); } while (0)
#define SB0() __builtin_amdgcn_sched_barrier(0)
DEVI float bf_lo(unsigned u) { return __uint_as_float(u << 16); }
DEVI float bf_hi(unsigned u) { return __uint_as_float(u & 0xffff0000u); }
DEVI float rbf(float v) { return (float)(__bf16)v; }
DEVI unsigned pk_bf(float a, float b) { bf2 r = {(__bf16)a, (__bf16)b}; return __builtin_bit_cast(unsigned, r); }
DEVI f4 mfma(bf8 a, bf8 b, f4 c) { return __builtin_amdgcn_mfma_f32_16x16x32_bf16(a, b, c, 0, 0, 0); }
DEVI void st128(u4 v, srd_t srd, unsigned voff, unsigned soff) {
  __builtin_amdgcn_raw_buffer_store_b128(v, srd, voff, soff, 16);
  asm volatile("s_nop 1" ::"v"(v) : "memory");
}
DEVI bf8 asb(u4 v) { return __builtin_bit_cast(bf8, v); }
DEVI f2 lo2(f4 v) { return __builtin_shufflevector(v, v, 0, 1); }
DEVI f2 hi2(f4 v) { return __builtin_shufflevector(v, v, 2, 3); }
DEVI f2 fma2(f2 a, f2 b, f2 c) { return __builtin_elementwise_fma(a, b, c); }
DEVI f2 sp(float a) { return f2{a, a}; }
DEVI u2 swap16(unsigned a) { auto r = __builtin_amdgcn_permlane16_swap(a, a, false, false); return u2{r[0], r[1]}; }
DEVI u2 swap32(unsigned a) { auto r = __builtin_amdgcn_permlane32_swap(a, a, false, false); return u2{r[0], r[1]}; }
DEVI float hsum(float v) {
  const unsigned u = __float_as_uint(v);
  auto b = swap32(u);
  return __uint_as_float(b[0]) + __uint_as_float(b[1]);
}
DEVI float gsum(float v) {
  const unsigned u = __float_as_uint(v);
  auto a = swap16(u);
  return hsum(__uint_as_float(a[0]) + __uint_as_float(a[1]));
}
template <int CTRL> DEVI float dpp_add(float v) {
  return v + __uint_as_float(__builtin_amdgcn_update_dpp(0u, __float_as_uint(v), CTRL, 0xf, 0xf, true));
}
DEVI float wave_sum(float v) {
  v = dpp_add<0xB1>(v);
  v = dpp_add<0x4E>(v);
  v = dpp_add<0x141>(v);
  v = dpp_add<0x140>(v);
  return gsum(v);
}
DEVI void gather4(float v, float& g0, float& g1, float& g2, float& g3) {
  unsigned u = __float_as_uint(v);
  auto a = swap16(u);
  auto e = swap32(a[0]);
  auto o = swap32(a[1]);
  g0 = __uint_as_float(e[0]); g2 = __uint_as_float(e[1]); g1 = __uint_as_float(o[0]); g3 = __uint_as_float(o[1]);
}
struct cf { float r, i; };
DEVI float opq(float v) { asm("" : "+v"(v)); return v; }
DEVI cf cmul(cf a, cf b) { return {fmaf(a.r, b.r, opq(-a.i * b.i)), fmaf(a.r, b.i, a.i * b.r)}; }
DEVI cf csq(cf a) { return {fmaf(a.r, a.r, opq(-a.i * a.i)), opq(a.r + a.r) * a.i}; }
DEVI cf cfma(cf a, cf b, cf c) { return {fmaf(a.r, b.r, opq(fmaf(-a.i, b.i, c.r))), fmaf(a.r, b.i, fmaf(a.i, b.r, c.i))}; }
DEVI int tokmap(int m, int r) { return 16 * (r >> 2) + 8 * (r & 1) + 2 * m + ((r >> 1) & 1); }

constexpr unsigned SPIN_CAP = 1u << 18;

constexpr int WIN0 = 3, WINF = 5, WINS = 5, NCA = 3;
DEVI void nap() { __builtin_amdgcn_s_sleep(4); asm volatile("" ::: "memory"); }
DEVI unsigned lds_ld(LAS char* a) { return __hip_atomic_load((LAS unsigned*)a, __ATOMIC_RELAXED, __HIP_MEMORY_SCOPE_WORKGROUP); }
DEVI void lds_st(LAS char* a, unsigned v) { __hip_atomic_store((LAS unsigned*)a, v, __ATOMIC_RELAXED, __HIP_MEMORY_SCOPE_WORKGROUP); }
DEVI cf sel4(int g, cf a, cf b, cf c, cf d) { return {g == 0 ? a.r : g == 1 ? b.r : g == 2 ? c.r : d.r, g == 0 ? a.i : g == 1 ? b.i : g == 2 ? c.i : d.i}; }
struct Ssm { cf lam, coef2, l8, l16, l64; };
DEVI f4 ssm_base(unsigned short plre, unsigned short plim, unsigned short pldt) {
  const float dt = rbf(expf(bf_lo(pldt))), a = bf_lo(plre), b = bf_lo(plim);
  const float e = expf(dt * a);
  float sn, cn; sincosf(dt * b, &sn, &cn);
  const float lr = e * cn, li = e * sn, nr = lr - 1.f, den = 2.0f / (a * a + b * b);
  return f4{lr, li, fmaf(nr, a, opq(li * b)) * den, fmaf(li, a, -nr * b) * den};
}

#if defined(__HIP_DEVICE_COMPILE__)
#define KARG __attribute__((address_space(4)))
#else
#define KARG
#endif
DEVI constexpr unsigned slotoff(int sl) { return (unsigned)(((sl / 3 + 2) % 3) * STG + (sl % 3) * 4096); }
#define LANEVARS() \
  int tid = threadIdx.x; asm volatile("" : "+v"(tid)); \
  const int lane = tid & 63, r16 = lane & 15, g = lane >> 4; \
  const unsigned q4 = r16 >> 2, p4 = lane & 3; \
  const unsigned vD = (unsigned)((lane >> 3) * 1024 + n64 * 128 + ((((lane & 7) >> 1) ^ g) * 32) + (lane & 1) * 16); \
  auto fillp = [&](int j, int sl, int i) { glds16(slotoff(sl) + q * 12288 + i * 1024, vD, sD, (unsigned)((11 * q + j) * 32 + 8 * i) * 1024u); }; \
  auto fill = [&](int j) { fillp(j, j, 0); fillp(j, j, 1); fillp(j, j, 2); fillp(j, j, 3); }; \
  auto pollflags = [&](unsigned base, int n, unsigned stride) { \
    unsigned a_ = lane < n ? base + lane * stride : OOB; \
    _Pragma("unroll 1") for (unsigned spins = 0; spins < SPIN_CAP; ++spins) { \
      const unsigned f = __builtin_amdgcn_raw_buffer_load_b32(sWS, a_, 0, 16); \
      if (f == tag) a_ = OOB; \
      if (__all(a_ == OOB)) break; \
      nap(); \
    } \
    asm volatile("" ::: "memory"); \
  };
#define PREAMBLE() \
  const KARG Params* kp = (const KARG Params*)(uintptr_t)__builtin_amdgcn_kernarg_segment_ptr(); asm volatile("" : "+s"(kp)); \
  const Params p = *kp; \
  int bid = blockIdx.x, w = w_; asm volatile("" : "+s"(bid), "+s"(w)); \
  const int q = w & 3, xcd = bid & 7, slot = bid >> 3; \
  const srd_t sWS = mk_srd(p.ws, WS_END); \
  const int tb = (xcd >> 2) * 8 + (slot >> 2), cg = (xcd & 3) * 4 + (slot & 3); \
  const int t2 = xcd * 4 + (slot >> 3), n64 = slot & 7; \
  const int wm = q & 1, wn = q >> 1, t64 = tb * 2 + wm; \
  const bool has3 = (cg & 3) < 3 && cg != 14; \
  const int fb0 = 11 * (cg >> 2) + 3 * (cg & 3) - (cg == 15); \
  const srd_t sX = mk_srd(p.x, SEQ * DM * 2); \
  const srd_t sD = mk_srd(p.wd, FF * DM * 2); \
  unsigned tag; \
  f4 acc2[4][4] = {};
DEVI bf8 cat(bf4 a, bf4 b) { return __builtin_shufflevector(a, b, 0, 1, 2, 3, 4, 5, 6, 7); }
DEVI void lds_wait(LAS char* a, unsigned n) { while (lds_ld(a) < n) __builtin_amdgcn_s_sleep(1); asm volatile("" ::: "memory"); }
DEVI void lds_add64(unsigned addr) {
  unsigned long long keep;
  asm volatile("s_waitcnt lgkmcnt(0)\n\ts_mov_b64 %0, exec\n\ts_mov_b64 exec, 1\n\tds_add_u32 %1, %2\n\ts_mov_b64 exec, %0" : "=&s"(keep) : "v"(addr), "v"(64u) : "memory");
}
template <unsigned N> DEVI void lds_spin(unsigned addr) {
  unsigned v, sv;
  asm volatile("1:\n\tds_read_b32 %0, %2\n\ts_waitcnt lgkmcnt(0)\n\tv_readfirstlane_b32 %1, %0\n\ts_nop 1\n\ts_cmp_ge_u32 %1, %3\n\ts_cbranch_scc0 1b"
               : "=&v"(v), "=&s"(sv) : "v"(addr), "i"(N) : "memory", "scc");
}
DEVI void lds_post(LAS char* a) { asm volatile("s_waitcnt lgkmcnt(0)" ::: "memory"); lds_st(a, 1u); }
DEVI f4 unpk(u2 v) { return f4{bf_lo(v.x), bf_hi(v.x), bf_lo(v.y), bf_hi(v.y)}; }

__global__ __launch_bounds__(512, 2) void s5_glu_kernel(Params p_) {
  __shared__ __attribute__((aligned(16))) char smem_[LDS_BYTES];
  LAS char* smem = (LAS char*)smem_;
  auto rd128 = [&](unsigned addr) -> bf8 { return *(LAS bf8*)(smem + addr); };
  auto rdtr = [&](unsigned addr) -> bf4 { return __builtin_amdgcn_ds_read_tr16_b64_v4bf16((LAS bf4*)(smem + addr)); };
  asm volatile(".Ls5_start:");
  const int w_ = __builtin_amdgcn_readfirstlane(threadIdx.x >> 6);
  {
    unsigned long long pc; unsigned len, back;
    asm volatile("s_getpc_b64 %0\n.Ls5_pf_here:\n\ts_mov_b32 %1, .Ls5_end_a-.Ls5_start\n\ts_max_i32 %1, %1, .Ls5_end_b-.Ls5_start\n\ts_mov_b32 %2, .Ls5_pf_here-.Ls5_start" : "=s"(pc), "=s"(len), "=s"(back)::"scc");
    const srd_t sI = mk_srd((const void*)(pc - back), w_ == 0 ? len : 0u);
    glds4(L_JUNK, (unsigned)(((threadIdx.x & 63) * 32 + (blockIdx.x >> 3)) * 128), sI, 0);
  }
  if (w_ >= 4) {
    PREAMBLE()
    LANEVARS()
    const unsigned ch = (unsigned)(cg * 32 + wn * 16 + r16);
#define NEXT(n) do { waitv<n>(); BAR(); } while (0)
    Ssm K; cf l512, pwg, S0, S1, wT;
    auto consts_a = [&]() {
      const f4 k0 = *(LAS f4*)(smem + L_CP + (wn * 16 + r16) * 16);
      K.lam = {k0[0], opq(k0[1])}; K.coef2 = {k0[2], opq(k0[3])};
      K.l8 = csq(csq(csq(K.lam))); K.l16 = csq(K.l8); K.l64 = csq(csq(K.l16));
      asm volatile("" : "+v"(K.lam.r), "+v"(K.lam.i), "+v"(K.coef2.r), "+v"(K.coef2.i), "+v"(K.l8.r), "+v"(K.l8.i), "+v"(K.l16.r), "+v"(K.l16.i), "+v"(K.l64.r), "+v"(K.l64.i));
    };
    auto consts_b = [&]() {
      const cf l32 = csq(K.l16), l48 = cmul(l32, K.l16), l128 = csq(K.l64), l256 = csq(l128), l384 = cmul(l256, l128);
      l512 = csq(l256); pwg = sel4(g, cf{1.f, 0.f}, l128, l256, l384); wT = sel4(g, l48, l32, K.l16, cf{1.f, 0.f});
      S0 = cmul(K.coef2, sel4(g, cf{1.f, 0.f}, K.l16, l32, l48)); S1 = cmul(K.l8, S0);
      asm volatile("" : "+v"(l512.r), "+v"(l512.i), "+v"(pwg.r), "+v"(pwg.i), "+v"(S0.r), "+v"(S0.i), "+v"(S1.r), "+v"(S1.i), "+v"(wT.r), "+v"(wT.i));
    };
    if (q < 3) {
      const srd_t sa = mk_srd(q == 0 ? p.x : q == 1 ? p.wg : p.wu, q == 0 ? SEQ * DM * 2 : DM * FF * 2);
      const srd_t sb = mk_srd(q == 0 ? p.x : p.bre, q == 0 ? SEQ * DM * 2 : DM * DM * 2), sc = mk_srd(q == 0 ? p.x : p.bim, q == 0 ? SEQ * DM * 2 : DM * DM * 2);
      const unsigned stepa = q == 0 ? 128u : 64u * FF * 2;
      unsigned ma = q == 0 ? L_A : q == 1 ? L_WG : L_WU, mb = q == 0 ? L_A + 6144u : L_SB + (q - 1) * 4096u;
      unsigned vo[6], vb[2];
      const unsigned da = q == 0 ? 65536u : 32u * FF * 2, la = q == 0 ? 8192u : 6144u, db = q == 0 ? 65536u : 0u, lb = q == 0 ? 8192u : 2048u;
#pragma unroll
      for (int i = 0; i < 2; ++i) {
        const int r = 8 * i + (lane >> 3);
        vb[i] = (unsigned)(q == 0 ? tb * 128 + tokmap(3, r) : cg * 32 + (q - 1) * 16 + r) * 1024u + (unsigned)(((lane & 7) ^ ((r >> 1) & 7)) * 16);
      }
      auto pc1 = [&](int i, int s) {
        if (i < 12) glds16((s % 3) * STG + ma + (i / 6) * la + (i % 6) * 1024, vo[i % 6], sa, stepa * s + (i / 6) * da);
        else glds16((s % 3) * STG + mb + ((i - 12) >> 1) * lb + (i & 1) * 1024, vb[i & 1], i < 14 ? sb : sc, 128u * s + ((i - 12) >> 1) * db);
      };
      pc1(12, 0); pc1(13, 0); pc1(14, 0); pc1(15, 0);
#pragma unroll
      for (int i = 0; i < 3; ++i) {
        const int idx = 64 * i + lane, k = idx / 12, c = idx - 12 * k, u = (c >> 1) ^ ((k >> 3) & 1);
        const unsigned vw = (!has3 && u >= 4) ? OOB : (unsigned)(k * (FF * 2) + (fb0 * 32 + 16 * u + 8 * (c & 1)) * 2);
        vo[i] = q == 0 ? vb[i & 1] - (3 - (i >> 1)) * 2048u : vw;
        vo[i + 3] = q == 0 ? vb[(i + 1) & 1] - (3 - ((i + 3) >> 1)) * 2048u : vw + 16u * FF * 2;
        pc1(i, 0); pc1(i + 3, 0); pc1(i + 6, 0); pc1(i + 9, 0);
      }
      auto eight = [&](int hf, int s) {
        asm volatile("" : "+s"(ma), "+s"(mb));
#pragma unroll
        for (int j = 0; j < 8; ++j) pc1(j < 6 ? hf * 6 + j : 12 + hf * 2 + (j - 6), s);
      };
      auto fills = [&](int sl0) {
#pragma unroll
        for (int i = 0; i < 16; ++i) glds16(slotoff(sl0) + q * 4096 + (i >> 2) * 12288 + (i & 3) * 1024, vD, sD, (unsigned)((11 * (i >> 2) + sl0 + q) * 32 + 8 * (i & 3)) * 1024u);
      };
      eight(0, 1);
      asm volatile("s_waitcnt vmcnt(8)" ::: "memory");
      BAR();
      eight(1, 1); eight(0, 2); NEXT(8);
      eight(1, 2); eight(0, 3); NEXT(8);
      eight(1, 3); eight(0, 4); eight(1, 4); NEXT(16);
      eight(0, 5); eight(1, 5); consts_a(); NEXT(16);
      eight(0, 6); eight(1, 6); consts_b(); NEXT(16);
      eight(0, 7); eight(1, 7); NEXT(16);
      asm volatile("s_waitcnt vmcnt(0)" ::: "memory");
      lds_add64(L_CNT + 8);
      fills(0);
      lds_wait(smem + L_CNT + 12, 256u);
      fills(3);
      tag = __builtin_amdgcn_readfirstlane(lds_ld(smem + L_CNT + 4));
    } else {
      {
        const unsigned c2 = (unsigned)(cg * 32 + (lane & 31)) * 2u;
        *(LAS f4*)(smem + L_CP + (lane & 31) * 16) = ssm_base(__builtin_amdgcn_raw_buffer_load_b16(mk_srd(p.lre, 1024), c2, 0, 0), __builtin_amdgcn_raw_buffer_load_b16(mk_srd(p.lim, 1024), c2, 0, 0),
                                                              __builtin_amdgcn_raw_buffer_load_b16(mk_srd(p.ldt, 1024), c2, 0, 0));
        asm volatile("" ::: "memory");
      }
      const int jx = xcd & 3, jn = jx == 3 ? 10 : 11, ji = ((slot & 3) ^ 3) * 8 + (slot >> 2), jmat = ji >= jn, jl = ji - jn * jmat, jkh = xcd >> 2;
      const int je = 6 - (jx & 1), jln = jl < je ? (11 * jx + 1) / 2 + jl : 22 + (11 * jx) / 2 + (jl - je);
      const bool jon = ji < 2 * jn;
      const int cn64 = bid & 7, cks = bid >> 3;
      u4 wj[16];
      {
        const srd_t sJ = mk_srd(jmat ? p.wu : p.wg, jon ? DM * FF * 2 : 0);
        const unsigned vj = (unsigned)((lane >> 3) * 5504 + (lane & 7) * 16), sj = (unsigned)(jln * 128), s0 = (jkh ? 128u : 64u) * 5504u, s1 = (jkh ? 192u : 0u) * 5504u;
#pragma unroll
        for (int i = 0; i < 8; ++i) wj[i] = __builtin_amdgcn_raw_buffer_load_b128(sJ, vj, sj + s0 + i * 8 * 5504, 0);
        BAR();
#pragma unroll
        for (int i = 0; i < 8; ++i) wj[8 + i] = __builtin_amdgcn_raw_buffer_load_b128(sJ, vj, sj + s1 + i * 8 * 5504, 0);
        BAR();
      }
      tag = __builtin_amdgcn_readfirstlane(lds_ld(smem + L_CNT + 4));
      consts_a();
      BAR();
      {
        u4 sel;
#pragma unroll
        for (int i = 0; i < 4; ++i) sel[i] = (r16 >> 1) == i ? ((r16 & 1) ? 0x3f800000u : 0x00003f80u) : 0u;
        f4 d = {0.f, 0.f, 0.f, 0.f};
        asm volatile("" : "+v"(sel) :: "memory");
#pragma unroll
        for (int i = 0; i < 8; ++i) d = mfma(asb(wj[i]), asb(sel), d);
        asm volatile("" : "+v"(d));
        BAR();
#pragma unroll
        for (int i = 8; i < 16; ++i) d = mfma(asb(wj[i]), asb(sel), d);
        const unsigned so = jon ? WS_CSW + (unsigned)((jmat * 2 + jkh) * 2752 + jln * 64) * 8u : OOB;
#pragma unroll
        for (int j = 0; j < 4; ++j) __builtin_amdgcn_raw_buffer_store_b64(u2{__float_as_uint(hsum(d[j])), tag}, sWS, (lane & 0x28) == 0 ? (unsigned)(8 * (4 * g + j) + r16) * 8u : OOB, so, 16);
      }
      BAR();
      const unsigned gb = WS_STATS + (unsigned)(tb * 128 + 2 * lane) * 8u, gc = WS_CSB + (unsigned)(((lane >> 4) & 1) * 512 + cg * 32 + 2 * r16) * 8u;
      u4 g0 = __builtin_amdgcn_raw_buffer_load_b128(sWS, gb, 0, 16), g1 = __builtin_amdgcn_raw_buffer_load_b128(sWS, gc, 0, 16);
      consts_b();
      BAR();
#pragma unroll 1
      for (unsigned spins = 0; spins < SPIN_CAP && !__all((g0.y == tag) & (g0.w == tag) & (g1.y == tag) & (g1.w == tag)); ++spins) {
        nap();
        g0 = __builtin_amdgcn_raw_buffer_load_b128(sWS, gb, 0, 16); g1 = __builtin_amdgcn_raw_buffer_load_b128(sWS, gc, 0, 16);
        asm volatile("" : "+v"(g0), "+v"(g1));
      }
#pragma unroll
      for (int s = 0; s < 2; ++s) {
        const unsigned v = s ? g0.z : g0.x;
        const int rho = (lane & 32) * 2 + (lane & 3) * 16 + ((lane >> 3) & 3) * 4 + 2 * s + ((lane >> 2) & 1);
        const float rs = __builtin_amdgcn_rcpf(bf_hi(v)), nm = -bf_lo(v);
        *(LAS float*)(smem + L_RS + rho * 4) = rs;
        *(LAS float*)(smem + L_NM + rho * 4) = rs * nm;
        *(LAS float*)(smem + L_PR + rho * 4) = nm;
      }
      *(LAS u2*)(smem + L_CSB + (lane & 31) * 8) = u2{g1.x, g1.z};
      nap();
      glds4(L_JUNK, lane < 43 ? (unsigned)(bid * 43 + lane) * 128u : OOB, sD, 0);
#pragma unroll
      for (int i = 0; i < 4; ++i) {
        const int r = 8 * (i & 1) + (lane >> 3);
        glds16(L_TR + i * 1024, (unsigned)(r * 1024 + ((lane & 7) ^ ((r >> 1) & 7)) * 16), mk_srd((cks & 2) ? p.cim : p.cre, DM * DM * 2), (unsigned)((cn64 * 64 + (cks & 1) * 32 + (i >> 1) * 16) * 1024 + (cks >> 2) * 128));
      }
      glds4_sc1(L_JUNK, ((lane & 32) ? WS_FLS : WS_FLF) + (unsigned)(t2 * 32 + (lane & 31)) * 128u, sWS, 0);
      glds4_sc1(L_JUNK, (lane >> 2) < tb ? WS_AGG + (unsigned)((lane >> 2) * 8192 + cg * 512 + (lane & 3) * 128) : OOB, sWS, 0);
      NEXT(63);
      u4 gw[8];
      const unsigned ga = lane < (has3 ? 48 : 32) ? WS_CSW + (unsigned)(fb0 * 32 + 2 * lane) * 8u : OOB;
#pragma unroll
      for (int i = 0; i < 8; ++i) gw[i] = __builtin_amdgcn_raw_buffer_load_b128(sWS, ga, i * 1376 * 8, 16);
#pragma unroll 1
      for (unsigned spins = 0; spins < SPIN_CAP; ++spins) {
        bool ok = true;
#pragma unroll
        for (int i = 0; i < 8; ++i) ok &= (gw[i].y == tag) & (gw[i].w == tag);
        if (__builtin_expect(__all(ok | (ga == OOB)), 1)) break;
        nap();
#pragma unroll
        for (int i = 0; i < 8; ++i) gw[i] = __builtin_amdgcn_raw_buffer_load_b128(sWS, ga, i * 1376 * 8, 16);
      }
      if (lane < 48) {
#pragma unroll
        for (int m = 0; m < 2; ++m)
          *(LAS f2*)(smem + L_CS + m * 384 + lane * 8) = f2{(__uint_as_float(gw[4 * m].x) + __uint_as_float(gw[4 * m + 1].x)) + (__uint_as_float(gw[4 * m + 2].x) + __uint_as_float(gw[4 * m + 3].x)),
                                                            (__uint_as_float(gw[4 * m].z) + __uint_as_float(gw[4 * m + 1].z)) + (__uint_as_float(gw[4 * m + 2].z) + __uint_as_float(gw[4 * m + 3].z))};
      }
      lds_post(smem + L_CNT + 84);
      asm volatile("" ::: "memory");
#pragma unroll
      for (int i = 0; i < 4; ++i)
        st128(__builtin_bit_cast(u4, rd128(L_TR + (i >> 1) * 2048 + ((unsigned)(r16 * 128 + ((g ^ (r16 >> 1)) * 16)) ^ ((i & 1) * 64)))), sWS, lane * 16,
              WS_CT + (unsigned)(((cn64 * 32 + (cks >> 2) * 4 + (i & 1) * 2 + ((cks >> 1) & 1)) * 4 + (cks & 1) * 2 + (i >> 1)) * 1024));
      waitv<63>();
      lds_st(smem + L_CNT + 88, 1u);
    }
    u4 cfr[8][4];
    auto ldC = [&](int j) {
#pragma unroll
      for (int nt = 0; nt < 4; ++nt) cfr[j][nt] = __builtin_amdgcn_raw_buffer_load_b128(sWS, lane * 16 + nt * 1024, WS_CT + (unsigned)(n64 * 32 + q * 8 + j) * 4096u, 16);
    };
    f4 rs[4], br[4], bi[4];
    {
      const float csr = *(LAS float*)(smem + L_CSB + (wn * 16 + r16) * 4), csi = opq(*(LAS float*)(smem + L_CSB + (32 + wn * 16 + r16) * 4));
#pragma unroll
      for (int m = 0; m < 4; ++m) {
        rs[m] = *(LAS f4*)(smem + L_RS + (wm * 64 + 16 * m + 4 * g) * 4);
        const f4 t = *(LAS f4*)(smem + L_NM + (wm * 64 + 16 * m + 4 * g) * 4);
        br[m] = t * csr; bi[m] = t * csi;
        asm volatile("" : "+v"(br[m]), "+v"(bi[m]), "+v"(rs[m]));
      }
    }
    __builtin_amdgcn_s_setprio(3);
    f2 vr[8], vi[8];
    const f2 lr2 = sp(K.lam.r), li2 = sp(K.lam.i);
    lds_wait(smem + L_CNT + 16 + q * 4, 1u);
#pragma unroll
    for (int m = 0; m < 4; ++m) {
      const f4 xr = *(LAS f4*)(smem + STG + q * 12288 + m * 1024 + lane * 16), xi = *(LAS f4*)(smem + STG + q * 12288 + 4096 + m * 1024 + lane * 16);
      vr[2 * m] = fma2(lo2(rs[m]), lo2(xr), lo2(br[m])); vr[2 * m + 1] = fma2(hi2(rs[m]), hi2(xr), hi2(br[m]));
      vi[2 * m] = fma2(lo2(rs[m]), lo2(xi), lo2(bi[m])); vi[2 * m + 1] = fma2(hi2(rs[m]), hi2(xi), hi2(bi[m]));
    }
    f2 hr = vr[0], hi = vi[0];
#pragma unroll
    for (int k = 1; k < 8; ++k) {
      const f2 nr = fma2(lr2, hr, fma2(-li2, hi, vr[k])), ni = fma2(lr2, hi, fma2(li2, hr, vi[k]));
      hr = nr; hi = ni;
    }
    const cf runA = {hr.x, hi.x};
    const cf agg = cfma(K.l8, runA, cf{hr.y, hi.y});
    cf T = cmul(wT, agg);
    T = {gsum(T.r), gsum(T.i)};
    LAS char* car = smem + L_CAR + wn * 256 + r16 * 8;
    if (wm == 0) {
      *(LAS f2*)car = f2{T.r, T.i};
      lds_post(smem + L_CNT + 48 + wn * 4);
      asm volatile("s_waitcnt vmcnt(0)" ::: "memory");
    } else {
      lds_wait(smem + L_CNT + 48 + wn * 4, 1u);
      const f2 t0 = *(LAS f2*)car;
      const cf W = cfma(K.l64, cf{t0.x, opq(t0.y)}, T);
      st128(u4{__float_as_uint(W.r), tag, __float_as_uint(W.i), tag}, sWS, g == 0 ? ch * 16u : OOB, WS_AGG + (unsigned)tb * 8192u);
      asm volatile("s_waitcnt vmcnt(1)" ::: "memory");
    }
    if (lane == 0) __hip_atomic_fetch_add((LAS unsigned*)(smem + L_CNT + 32), 1u, __ATOMIC_RELAXED, __HIP_MEMORY_SCOPE_WORKGROUP);
    __builtin_amdgcn_raw_buffer_store_b32(tag, sWS, (q == 3 && lane == 0) ? WS_FLC + (unsigned)((bid & 7) * 32 + (bid >> 3)) * 4u : OOB, 0, 16);
    asm volatile("" ::: "memory");
    __builtin_amdgcn_s_setprio(0);
    const f2 cr2 = sp(K.coef2.r), ci2 = sp(K.coef2.i);
#pragma unroll
    for (int k = 0; k < 8; ++k) {
      const f2 a = fma2(cr2, vr[k], -(ci2 * vi[k])), b = -fma2(cr2, vi[k], ci2 * vr[k]);
      vr[k] = a; vi[k] = b;
      asm volatile("" : "+v"(vr[k]), "+v"(vi[k])::"memory");
    }
    cf E0, E1;
    {
      cf A0, A1, A2, A3;
      gather4(agg.r, A0.r, A1.r, A2.r, A3.r);
      gather4(agg.i, A0.i, A1.i, A2.i, A3.i);
      const cf P2 = cfma(K.l16, A0, A1), P3 = cfma(K.l16, P2, A2);
      E0 = cmul(K.coef2, sel4(g, cf{0.f, 0.f}, A0, P2, P3)); E1 = cfma(K.l8, E0, cmul(K.coef2, runA));
      asm volatile("" : "+v"(E0.r), "+v"(E0.i), "+v"(E1.r), "+v"(E1.i));
    }
    __builtin_amdgcn_s_setprio(3);
    cf carry;
    if (wm == 0) {
      u4 Xg[4]; unsigned ad[4];
#pragma unroll
      for (int i = 0; i < 4; ++i) { const int src = tb - 1 - (4 * i + g); ad[i] = src >= 0 ? WS_AGG + (unsigned)src * 8192u + ch * 16u : OOB; }
      unsigned todo = 0xfu;
#pragma unroll 1
      for (unsigned spins = 0; todo && spins < SPIN_CAP; ++spins) {
#pragma unroll
        for (int i = 0; i < 4; ++i) if (todo >> i & 1) Xg[i] = __builtin_amdgcn_raw_buffer_load_b128(sWS, ad[i], 0, 16);
        unsigned left = 0;
#pragma unroll
        for (int i = 0; i < 4; ++i) left |= __all((ad[i] == OOB) | ((Xg[i].y == tag) & (Xg[i].w == tag))) ? 0u : 1u << i;
        todo = left;
        if (todo) nap();
      }
      cf c = {__uint_as_float(Xg[3].x), __uint_as_float(Xg[3].z)};
#pragma unroll
      for (int i = 2; i >= 0; --i) c = cfma(l512, c, cf{__uint_as_float(Xg[i].x), __uint_as_float(Xg[i].z)});
      c = cmul(c, pwg);
      carry = {gsum(c.r), gsum(c.i)};
      const cf c1 = cfma(K.l64, carry, T);
      *(LAS f2*)(car + 128) = f2{c1.r, c1.i};
      lds_post(smem + L_CNT + 56 + wn * 4);
      if (tb == 15 && cg == 0 && wn == 0 && lane == 0) __builtin_amdgcn_raw_buffer_store_b32(tag, sWS, WS_EPOCH, 0, 16);
    } else {
      lds_wait(smem + L_CNT + 56 + wn * 4, 1u);
      const f2 c1 = *(LAS f2*)(car + 128);
      carry = {c1.x, opq(c1.y)};
    }
    {
      lds_wait(smem + L_CNT + 88, 1u);
      carry = {opq(carry.r), opq(carry.i)};
      const cf h0 = cfma(S0, carry, E0), h1 = cfma(S1, carry, E1);
      hr = f2{h0.r, h1.r}; hi = f2{-h0.i, -h1.i};
      const unsigned so = WS_HS + (unsigned)(t64 * 32 + cg * 2) * 4096u + (unsigned)wn * 512u;
      const unsigned vo = (unsigned)((g >> 1) * 4096 + (lane & 31) * 16);
      const unsigned wa = L_TR + q * 2048 + r16 * 32 + g * 8, qa = L_TR + q * 2048 + (8 * g + q4) * 32 + p4 * 8;
      bf8 o;
#pragma unroll
      for (int m = 0; m < 4; ++m) {
        unsigned pr[2], pi[2];
#pragma unroll
        for (int j = 0; j < 2; ++j) {
          const int k = 2 * m + j;
          const f2 nr = fma2(lr2, hr, fma2(li2, hi, vr[k])), ni = fma2(lr2, hi, fma2(-li2, hr, vi[k]));
          hr = nr; hi = ni;
          pr[j] = pk_bf(hr.x, hr.y); pi[j] = pk_bf(hi.x, hi.y);
        }
        if (m > 0) st128(__builtin_bit_cast(u4, o), sWS, vo + (m - 1) * 1024, so);
        *(LAS u2*)(smem + wa + (m & 1) * 1024) = u2{pr[0], pr[1]};
        *(LAS u2*)(smem + wa + (m & 1) * 1024 + 512) = u2{pi[0], pi[1]};
        asm volatile("" ::: "memory");
        o = cat(rdtr(qa + (m & 1) * 1024), rdtr(qa + (m & 1) * 1024 + 128));
        asm volatile("" ::: "memory");
      }
      st128(__builtin_bit_cast(u4, o), sWS, vo + 3 * 1024, so);
    }
    asm volatile("s_waitcnt vmcnt(0)" ::: "memory");
    __builtin_amdgcn_raw_buffer_store_b32(tag, sWS, lane == 0 ? WS_FLS + (unsigned)(t64 * 32 + cg * 2 + wn) * 128u : OOB, 0, 16);
    __builtin_amdgcn_s_setprio(0);
    pollflags(WS_FLC + (unsigned)(n64 * 128 + q * 32), 8, 4u);
#pragma unroll
    for (int j = 0; j < NCA; ++j) ldC(j);
    nap();
    pollflags(WS_FLS + (unsigned)(t2 * 32 + q * 8) * 128u, 8, 128u);
    u4 af[8][4];
    const unsigned pw = q * 12288 + r16 * 256 + ((g ^ r16) * 16);
    auto lda = [&](int j, int m) {
      af[j][m] = __builtin_amdgcn_raw_buffer_load_b128(sWS, lane * 16 + m * 1024, WS_HS + (unsigned)(t2 * 32 + q * 8 + j) * 4096u, 16);
    };
    constexpr int AH = NCA - 1, NQ = (16 - NCA) * 4;
    auto ldq = [&](int n) {
      const int gq = n >> 2, r = gq - AH;
      if (gq < AH) lda(gq, n & 3);
      else if (r < 2 * (8 - NCA)) {
        if (r & 1) lda(AH + (r >> 1), n & 3);
        else cfr[NCA + (r >> 1)][n & 3] = __builtin_amdgcn_raw_buffer_load_b128(sWS, lane * 16 + (n & 3) * 1024, WS_CT + (unsigned)(n64 * 32 + q * 8 + NCA + (r >> 1)) * 4096u, 16);
      } else lda(AH + (8 - NCA) + r - 2 * (8 - NCA), n & 3);
    };
#pragma unroll
    for (int n = 0; n < WINS * 4; ++n) ldq(n);
    SB0();
#pragma unroll
    for (int j = 0; j < 8; ++j) {
      if (j == 7) lds_wait(smem + L_CNT + 64 + q * 4, 1u);
#pragma unroll
      for (int i = 0; i < 16; ++i) {
        if (j == 7 && i >= 5) { const int k = i - 5; *(LAS f4*)(smem + slotoff(2 + (k & 3)) + (pw ^ ((k >> 2) * 64))) = acc2[k >> 2][k & 3]; }
        acc2[i >> 2][i & 3] = mfma(asb(cfr[j][i >> 2]), asb(af[j][i & 3]), acc2[i >> 2][i & 3]);
        { const int n = WINS * 4 + j * 8 + (i >> 1); if ((i & 1) && n < NQ) ldq(n); }
        SB0();
      }
    }
#pragma unroll
    for (int k = 11; k < 16; ++k) *(LAS f4*)(smem + slotoff(2 + (k & 3)) + (pw ^ ((k >> 2) * 64))) = acc2[k >> 2][k & 3];
    waitv<63>();
    BAR();
    asm volatile(".Ls5_end_b:");
    __builtin_amdgcn_endpgm();
  } else {
    PREAMBLE()
    f4 ag[4][4] = {}, av[2][4] = {};
    {
      LANEVARS()
      {
        const int brow = (tb >> 3) * 512 + cg * 32 + (tb & 7) * 4 + q, xr = tb * 128 + cg * 8 + 2 * q;
        u4 xa[2];
#pragma unroll
        for (int i = 0; i < 2; ++i) xa[i] = __builtin_amdgcn_raw_buffer_load_b128(sX, lane * 16 + i * 1024, (unsigned)xr * 1024u, 0);
        const unsigned pfw = __builtin_amdgcn_raw_buffer_load_b32(mk_srd(q < 2 ? p.wg : p.wu, DM * FF * 2), lane < 6 ? (unsigned)((bid * 2 + (q & 1)) * 6 + lane) * 128u : OOB, 0, 0);
        const u4 br = __builtin_amdgcn_raw_buffer_load_b128(mk_srd((brow >> 9) ? p.bim : p.bre, DM * DM * 2), lane * 16, (unsigned)(brow & 511) * 1024u, 0);
        const unsigned ep = __builtin_amdgcn_raw_buffer_load_b32(sWS, 0, WS_EPOCH, 16);
        SB0();
        float mean[2], sd[2];
#pragma unroll
        for (int i = 0; i < 2; ++i) {
          float v[8];
#pragma unroll
          for (int j = 0; j < 4; ++j) { v[2 * j] = bf_lo(xa[i][j]); v[2 * j + 1] = bf_hi(xa[i][j]); }
          mean[i] = wave_sum(((v[0] + v[1]) + (v[2] + v[3])) + ((v[4] + v[5]) + (v[6] + v[7]))) * (1.0f / DM);
          float qq = 0.f;
#pragma unroll
          for (int j = 0; j < 8; ++j) { const float dd = v[j] - mean[i]; qq = fmaf(dd, dd, qq); }
          sd[i] = __builtin_amdgcn_sqrtf(rbf(rbf(wave_sum(qq) * (1.0f / DM)) + 1e-5f));
        }
        asm volatile("" ::"v"(pfw));
        tag = __builtin_amdgcn_readfirstlane(ep) + 1u;
        if (tid < 32) lds_st(smem + L_CNT + tid * 4, tid == 1 ? tag : 0u);
        st128(u4{pk_bf(mean[0], sd[0]), tag, pk_bf(mean[1], sd[1]), tag}, sWS, lane == 0 ? WS_STATS + (unsigned)xr * 8u : OOB, 0);
        float s = 0.f;
#pragma unroll
        for (int j = 0; j < 4; ++j) s += bf_lo(br[j]) + bf_hi(br[j]);
        s = wave_sum(s);
        __builtin_amdgcn_raw_buffer_store_b64(u2{__float_as_uint(s), tag}, sWS, lane == 0 ? WS_CSB + (unsigned)brow * 8u : OOB, 0, 16);
      }
      f4 as[2][4] = {};
      unsigned ad[10];
      const unsigned aA0 = (unsigned)(r16 * 128 + ((g ^ (r16 >> 1)) * 16));
      ad[0] = (aA0 + wm * 8192) ^ (wn * 4096); ad[1] = ad[0] ^ 64u; ad[2] = ad[0] ^ 4096u; ad[3] = ad[2] ^ 64u;
      ad[4] = L_SB + aA0 + wn * 4096; ad[5] = ad[4] ^ 64u;
      ad[8] = L_WG + (unsigned)((8 * g + q4) * 192 + 8 * p4 + (g & 1) * 32) + 128u; ad[9] = ad[8] ^ 32u;
      ad[6] = ad[8] - 128u + wn * 64u; ad[7] = ad[6] ^ 32u;
#pragma unroll
      for (int i = 0; i < 10; ++i) asm volatile("" : "+v"(ad[i]));
      bf8 fa[2][4], fs[2], fw[4], fv[2][4];
      auto wfrag = [&](int b, int i, unsigned off) -> bf8 {
        const unsigned a = ad[b + (i & 1)] + (i >> 1) * 12288 + off;
        return cat(rdtr(a), rdtr(a + 768));
      };
      auto step = [&](auto H_) {
        constexpr int H = decltype(H_)::v, c = H & 1, n = c ^ 1;
        constexpr int kk = (H + 1) & 1;
        if ((H & 1) && H < 13) { waitv<63>(); BAR(); }
        if (H == 13) { lds_add64(L_CNT + 12); lds_spin<192>(L_CNT + 8); }
        if (H == 15) lds_add64(L_CNT + 80);
        unsigned ho = 0;
        if (H == 15) { int t_ = threadIdx.x; asm volatile("" : "+v"(t_)); ho = STG + q * 12288 + wn * 2048 + (t_ & 63) * 16; }
        SB0();
#pragma unroll
        for (int i = 0; i < 32; ++i) {
          if (i < 8) as[i >> 2][i & 3] = mfma(fa[c][i & 3], fs[i >> 2], as[i >> 2][i & 3]);
          else if (i < 24) ag[i & 3][(i - 8) >> 2] = mfma(fw[(i - 8) >> 2], fa[c][i & 3], ag[i & 3][(i - 8) >> 2]);
          else av[i & 1][(i - 24) >> 1] = mfma(fv[c][(i - 24) >> 1], fa[c][i & 1], av[i & 1][(i - 24) >> 1]);
          if (H < 15) {
            if (i < 3) fa[n][i] = rd128(ad[(i & 2) + kk] + (i & 1) * 2048);
            else if (i == 3) fa[n][3] = rd128(ad[2 + kk] + 2048);
            else if (i == 4) fs[0] = rd128(ad[4 + kk]);
            else if (i == 8) fs[1] = rd128(ad[4 + kk] + 2048);
            else if (i == 6 || i == 10 || i == 14 || i == 18) fv[n][(i - 6) >> 2] = wfrag(8, (i - 6) >> 2, kk * 6144);
            else if (i == 12 || i == 16 || i == 20 || i == 24) fw[(i - 12) >> 2] = wfrag(6, (i - 12) >> 2, kk * 6144);
            if (kk == 1 && H < 14 && i >= 25 && i < 30) {
#pragma unroll
              for (int k = 2 * (i - 25); k < 2 * (i - 25) + 2; ++k) { ad[k] += ((H >> 1) % 3 == 2) ? -2 * STG : STG; asm volatile("" : "+v"(ad[k])); }
            }
          } else {
            if (i == 7) lds_spin<256>(L_CNT + 80);
            if (i >= 8 && i < 16) { const int k = i - 8; *(LAS f4*)(smem + ((k & 2) ? ho ^ 2048u : ho) + (k >> 2) * 4096 + (k & 1) * 1024) = as[k >> 2][k & 3]; }
            if (i == 16) lds_st(smem + L_CNT + 16 + q * 4, 1u);
          }
          SB0();
        }
      };
      waitv<63>(); BAR();
      fs[0] = rd128(ad[4]);
#pragma unroll
      for (int i = 0; i < 4; ++i) fa[0][i] = rd128(ad[i & 2] + (i & 1) * 2048);
      fs[1] = rd128(ad[4] + 2048);
#pragma unroll
      for (int i = 0; i < 4; ++i) fw[i] = wfrag(6, i, 0);
#pragma unroll
      for (int i = 0; i < 4; ++i) fv[0][i] = wfrag(8, i, 0);
      step(IC<0>{}); step(IC<1>{}); step(IC<2>{}); step(IC<3>{}); step(IC<4>{}); step(IC<5>{}); step(IC<6>{});
      step(IC<7>{}); step(IC<8>{}); step(IC<9>{}); step(IC<10>{}); step(IC<11>{}); step(IC<12>{}); step(IC<13>{}); step(IC<14>{}); step(IC<15>{});
    }
    LANEVARS()
    lds_wait(smem + L_CNT + 84, 1u);
    {
      auto glu_frag = [&](const f4 (&ac)[4], const f4 (&cs)[4], float st) -> u4 {
        const f2 nm = sp(st);
        unsigned o[4];
#pragma unroll
        for (int i = 0; i < 2; ++i) {
          f2 g0 = fma2(nm, lo2(cs[i]), lo2(ac[i])), g1 = fma2(nm, hi2(cs[i]), hi2(ac[i]));
          f2 u0 = fma2(nm, lo2(cs[2 + i]), lo2(ac[2 + i])), u1 = fma2(nm, hi2(cs[2 + i]), hi2(ac[2 + i]));
          g0 = f2{fmaxf(g0.x, 0.f), fmaxf(g0.y, 0.f)} * u0;
          g1 = f2{fmaxf(g1.x, 0.f), fmaxf(g1.y, 0.f)} * u1;
          o[2 * i] = pk_bf(g0.x, g0.y); o[2 * i + 1] = pk_bf(g1.x, g1.y);
        }
        return u4{o[0], o[1], o[2], o[3]};
      };
      f4 cs[4], cv[4]; float st[4];
#pragma unroll
      for (int i = 0; i < 4; ++i) {
        cs[i] = *(LAS f4*)(smem + L_CS + ((i >> 1) * 96 + wn * 32 + (i & 1) * 16 + g * 4) * 4);
        st[i] = opq(*(LAS float*)(smem + L_PR + (wm * 64 + r16) * 4 + ((i * 64) ^ (wn * 128))));
      }
#pragma unroll
      for (int i = 0; i < 4; ++i) cv[i] = *(LAS f4*)(smem + L_CS + ((i >> 1) * 96 + 64 + (i & 1) * 16 + g * 4) * 4);
      const unsigned so = WS_HF + (unsigned)(t64 * 43 + fb0) * 4096u;
      const unsigned vo = (unsigned)(lane * 16 + wn * 2048);
#pragma unroll
      for (int m = 0; m < 4; ++m) { st128(glu_frag(ag[m], cs, st[m]), sWS, (m < 2 ? vo : vo ^ 2048u) + (m & 1) * 1024, so + wn * 4096); SB0(); }
#pragma unroll
      for (int m = 0; m < 2; ++m) { st128(glu_frag(av[m], cv, st[m]), sWS, vo + m * 1024, has3 ? so + 8192 : OOB); SB0(); }
    }
    asm volatile("s_waitcnt vmcnt(0)" ::: "memory");
    __builtin_amdgcn_raw_buffer_store_b32(tag, sWS, lane == 0 ? WS_FLF + (unsigned)(t64 * 32 + cg * 2 + wn) * 128u : OOB, 0, 16);
    u2 xv[4], xd[4], stg[4]; float r2[4];
#pragma unroll
    for (int i = 0; i < 4; ++i) {
      const int tok = t2 * 64 + tokmap(q, 4 * i + g);
      xv[i] = __builtin_amdgcn_raw_buffer_load_b64(sX, (unsigned)(tok * 1024 + n64 * 128 + r16 * 8), 0, 0);
      stg[i] = __builtin_amdgcn_raw_buffer_load_b64(sWS, WS_STATS + (unsigned)tok * 8u, 0, 16);
    }
    const u2 dk = __builtin_amdgcn_raw_buffer_load_b64(mk_srd(p.dsk, DM * 2), (unsigned)(n64 * 128 + r16 * 8), 0, 0);
    lds_wait(smem + L_CNT + 32, 4u);
#pragma unroll
    for (int j = 6; j < 9; ++j) fill(j);
    const unsigned aD = q * 12288 + (4 * g + q4) * 128 + (((2 * g + (q4 >> 1)) & 3) * 32) + p4 * 8;
    auto bfrag = [&](int sl, int nt) -> bf8 { return cat(rdtr((aD ^ (nt * 32)) + slotoff(sl)), rdtr((aD ^ (nt * 32)) + slotoff(sl) + 2048)); };
    bf8 bc[4], bn[4];
    nap();
    pollflags(WS_FLF + (unsigned)(t2 * 32 + q * 8) * 128u, 8, 128u);
    if (__builtin_expect(!__all((stg[0].y == tag) & (stg[1].y == tag) & (stg[2].y == tag) & (stg[3].y == tag)), 0)) {
#pragma unroll
      for (int i = 0; i < 4; ++i) stg[i] = __builtin_amdgcn_raw_buffer_load_b64(sWS, WS_STATS + (unsigned)(t2 * 64 + tokmap(q, 4 * i + g)) * 8u, 0, 16);
      asm volatile("" : "+v"(stg[0]), "+v"(stg[1]), "+v"(stg[2]), "+v"(stg[3]));
    }
    u4 af[11][4];
    auto lda = [&](int j, int m) {
      af[j][m] = __builtin_amdgcn_raw_buffer_load_b128(sWS, lane * 16 + m * 1024, (j < 10 || q < 3) ? WS_HF + (unsigned)(t2 * 43 + 11 * q + j) * 4096u : OOB, 16);
    };
#pragma unroll
    for (int n = 0; n < WIN0 * 4; ++n) lda(n >> 2, n & 3);
    SB0();
    {
      const f4 ds = unpk(dk);
#pragma unroll
      for (int i = 0; i < 4; ++i) {
        const float mean = bf_lo(stg[i].x), rs = __builtin_amdgcn_rcpf(bf_hi(stg[i].x));
        const f4 xs = unpk(xv[i]);
        float d[4];
#pragma unroll
        for (int j = 0; j < 4; ++j) d[j] = rbf(rbf(xs[j] - mean) * rs) * ds[j];
        xd[i] = u2{pk_bf(d[0], d[1]), pk_bf(d[2], d[3])};
        r2[i] = rs * rs;
      }
      asm volatile("" : "+v"(xd[0]), "+v"(xd[1]), "+v"(xd[2]), "+v"(xd[3]), "+v"(xv[0]), "+v"(xv[1]), "+v"(xv[2]), "+v"(xv[3]));
    }
    SB0();
    {
      const unsigned pw = q * 12288 + r16 * 256 + ((g ^ r16) * 16);
#pragma unroll
      for (int nt = 0; nt < 4; ++nt) bc[nt] = bfrag(0, nt);
#pragma unroll
      for (int j = 0; j < 11; ++j) {
#pragma unroll
        for (int i = 0; i < 16; ++i) {
          acc2[i >> 2][i & 3] = mfma(bc[i >> 2], asb(af[j][i & 3]), acc2[i >> 2][i & 3]);
          if (j + 1 < 11 && i < 4) bn[i] = bfrag(j + 1 < 9 ? j + 1 : j + 1 - 9, i);
          {
            constexpr int R = WINF - WIN0;
            if (j < R) { if (i & 1) { const int n = (WIN0 + 2 * j) * 4 + (i >> 1); lda(n >> 2, n & 3); } }
            else if (j + WINF < 11 && (i & 3) == 3) lda(j + WINF, i >> 2);
          }
          if (j >= 1 && j <= 2 && (i & 3) == 2) fillp(8 + j, j - 1, i >> 2);
          if (j == 4 && i == 12) lds_post(smem + L_CNT + 64 + q * 4);
          if (j == 10 && i >= 4) { const int k = i - 4; *(LAS f4*)(smem + slotoff((k & 3) < 3 ? 6 + (k & 3) : 0) + (pw ^ ((k >> 2) * 64))) = acc2[k >> 2][k & 3]; }
          SB0();
        }
#pragma unroll
        for (int nt = 0; nt < 4; ++nt) bc[nt] = bn[nt];
      }
#pragma unroll
      for (int k = 12; k < 16; ++k) *(LAS f4*)(smem + slotoff((k & 3) < 3 ? 6 + (k & 3) : 0) + (pw ^ ((k >> 2) * 64))) = acc2[k >> 2][k & 3];
    }
    waitv<63>();
    if (lane == 0) __hip_atomic_fetch_add((LAS unsigned*)(smem + L_CNT), 1u, __ATOMIC_RELAXED, __HIP_MEMORY_SCOPE_WORKGROUP);
    lds_wait(smem + L_CNT, 4u);
    const unsigned pf0 = q == 0 ? slotoff(6) : q == 1 ? slotoff(7) : q == 2 ? slotoff(8) : slotoff(0);
    const unsigned ps0 = q == 0 ? slotoff(2) : q == 1 ? slotoff(3) : q == 2 ? slotoff(4) : slotoff(5);
    f4 G[4], O[4];
#pragma unroll
    for (int i = 0; i < 4; ++i) {
      const int r = 4 * i + g;
      LAS char* src = smem + pf0 + r * 256 + ((r16 ^ r) * 16);
      const f4 yf = (*(LAS f4*)(src) + *(LAS f4*)(src + 12288)) + (*(LAS f4*)(src + 24576) + *(LAS f4*)(src + 36864));
#pragma unroll
      for (int j = 0; j < 4; ++j) {
        const float f = rbf(yf[j] * r2[i]);
        G[i][j] = rbf(f * __builtin_amdgcn_rcpf(1.0f + __expf(-f)));
      }
      O[i] = unpk(xd[i]) * G[i] + unpk(xv[i]);
    }
    asm volatile("" : "+v"(G[0]), "+v"(G[1]), "+v"(G[2]), "+v"(G[3]), "+v"(O[0]), "+v"(O[1]), "+v"(O[2]), "+v"(O[3]));
    BAR();
#pragma unroll
    for (int i = 0; i < 4; ++i) {
      const int r = 4 * i + g;
      LAS char* src = smem + ps0 + r * 256 + ((r16 ^ r) * 16);
      const f4 ys = (*(LAS f4*)(src) + *(LAS f4*)(src + 12288)) + (*(LAS f4*)(src + 24576) + *(LAS f4*)(src + 36864));
      *(f4*)(p.out + (size_t)(t2 * 64 + tokmap(q, r)) * DM + n64 * 64 + r16 * 4) = ys * G[i] + O[i];
    }
    asm volatile(".Ls5_end_a:");
    __builtin_amdgcn_endpgm();
  }
}
}

extern "C" void kernel_launch(void* const* d_in, const int* in_sizes, int n_in, void* d_out, int out_size, void* d_ws,
                              size_t ws_size, hipStream_t stream) {
  Params p;
  p.x = d_in[0]; p.lre = d_in[1]; p.lim = d_in[2]; p.bre = d_in[3]; p.bim = d_in[4]; p.cre = d_in[5]; p.cim = d_in[6];
  p.ldt = d_in[7]; p.dsk = d_in[8]; p.wg = d_in[10]; p.wu = d_in[11]; p.wd = d_in[12];
  p.out = (float*)d_out; p.ws = (char*)d_ws;
  s5_glu_kernel<<<256, 512, 0, stream>>>(p);
}
```
